```python
import torch

def _softmax(x, axis=-1):
    return torch.softmax(x, dim=axis)
def _gelu(x):
    return torch.nn.functional.gelu(x, approximate="tanh")
def _silu(x):
    return torch.nn.functional.silu(x)
def _sigmoid(x):
    return torch.sigmoid(x)
def _layernorm(x, axis=-1, eps=1e-5):
    m = x.mean(dim=axis, keepdim=True)
    v = x.var(dim=axis, keepdim=True, unbiased=False)
    return (x - m) / torch.sqrt(v + eps)
def _scatter_add(tgt, idx, src, axis=0):
    return tgt.clone().index_add_(axis, idx, src)
def _segment_sum(src, seg, n):
    out = torch.zeros((n,) + src.shape[1:], dtype=src.dtype)
    return out.index_add_(0, seg, src)
def _pad(x, pw):
    flat = tuple(v for pair in reversed(pw) for v in pair)
    return torch.nn.functional.pad(x, flat)

kernel_name = 'sequential_preLN__linear_kernel_hedgehog__dense_activation_relu__prompt'

def setup_inputs():

    _w = (lambda a: (lambda t: t.to(torch.bfloat16) if t.is_floating_point() else t)(torch.as_tensor(a)))
    def _A_setup():
        B, S, D, H = 1, 2048, 640, 32
        Dh = D // H
        return {
            "x": _w((lambda *s: torch.randn(*s, dtype=torch.bfloat16) * 0.05)(B, S, D)),
            "wqkv": _w((lambda *s: torch.randn(*s, dtype=torch.bfloat16) * 0.2)(D, 3 * D)),
            "w_phi": _w((lambda *s: torch.randn(*s, dtype=torch.bfloat16) * 0.2)(H, Dh, Dh) * (Dh ** -0.5)),
            "wo": _w((lambda *s: torch.randn(*s, dtype=torch.bfloat16) * 0.05)(D, D)),
        }
    _A_in = _A_setup()

    def _B_setup():
        B, S, D, Dff = 1, 2048, 640, 1728
        return {
            "x": _w((lambda *s: torch.randn(*s, dtype=torch.bfloat16) * 0.05)(B, S, D)),
            "w1": _w((lambda *s: torch.randn(*s, dtype=torch.bfloat16) * 0.05)(D, Dff)),
            "w2": _w((lambda *s: torch.randn(*s, dtype=torch.bfloat16) * 0.05)(Dff, D)),
        }
    _B_in = _B_setup()

    return {
        "A_x": _A_in["x"],
        "A_wqkv": _A_in["wqkv"],
        "A_w_phi": _A_in["w_phi"],
        "A_wo": _A_in["wo"],
        "B_x": _B_in["x"],
        "B_w1": _B_in["w1"],
        "B_w2": _B_in["w2"],
    }

def reference(A_x, A_wqkv, A_w_phi, A_wo, B_x, B_w1, B_w2):
    def _A_ref(x, wqkv, w_phi, wo):
        B, S, D = x.shape
        H = 32; Dh = D // H
        qkv = (lambda x, *d: x.permute(*d))((x @ wqkv).reshape(B, S, 3, H, Dh), 2, 0, 3, 1, 4)
        q, k, v = qkv[0], qkv[1], qkv[2]
        pq = torch.einsum("bhsd,hdf->bhsf", q, w_phi)
        pk = torch.einsum("bhsd,hdf->bhsf", k, w_phi)
        phi_q = torch.concatenate([_softmax(pq, axis=-1), _softmax(-pq, axis=-1)], axis=-1)
        phi_k = torch.concatenate([_softmax(pk, axis=-1), _softmax(-pk, axis=-1)], axis=-1)
        kv = torch.einsum("bhsf,bhsd->bhfd", phi_k, v)
        z  = torch.einsum("bhsf->bhf", phi_k)
        num = torch.einsum("bhqf,bhfd->bhqd", phi_q, kv)
        den = torch.einsum("bhqf,bhf->bhq", phi_q, z)[..., None] + 1e-6
        o = num / den
        return (lambda x, *d: x.permute(*d))(o, 0, 2, 1, 3).reshape(B, S, D) @ wo

    def _B_ref(x, w1, w2):
        h = torch.einsum("bsd,df->bsf", x, w1)
        zero = torch.zeros_like(h)
        h = torch.maximum(h, zero)
        return torch.einsum("bsf,fd->bsd", h, w2)

    x = A_x
    h = x + _A_ref(_layernorm(x, axis=-1), A_wqkv, A_w_phi, A_wo)
    h2 = h + _B_ref(_layernorm(h, axis=-1), B_w1, B_w2)
    return h2
```

```cpp
#include <hip/hip_runtime.h>
#include <hip/hip_bf16.h>
typedef unsigned short u16;
static __device__ __forceinline__ float bf2f(u16 b){ return __uint_as_float(((unsigned)b)<<16); }
static __device__ __forceinline__ u16 f2bf(float f){ unsigned u=__float_as_uint(f); u += 0x7FFFu + ((u>>16)&1u); return (u16)(u>>16); }
constexpr int S=2048,D=640,H=32,DH=20,DFF=1728;

__global__ void k_ln(const u16* x, const float* xf, float* y){
  int r=blockIdx.x; __shared__ float red[256];
  float v[3]; int t=threadIdx.x; float s=0;
  for(int i=0;i<3;i++){int c=t+256*i; v[i]= c<D ? (x? bf2f(x[r*D+c]) : xf[r*D+c]) : 0.f; s+=v[i];}
  red[t]=s; __syncthreads(); for(int o=128;o>0;o>>=1){ if(t<o) red[t]+=red[t+o]; __syncthreads(); }
  float m=red[0]/D; __syncthreads(); float q=0;
  for(int i=0;i<3;i++){int c=t+256*i; if(c<D){float d=v[i]-m; q+=d*d;}}
  red[t]=q; __syncthreads(); for(int o=128;o>0;o>>=1){ if(t<o) red[t]+=red[t+o]; __syncthreads(); }
  float rs=rsqrtf(red[0]/D+1e-5f);
  for(int i=0;i<3;i++){int c=t+256*i; if(c<D) y[r*D+c]=(v[i]-m)*rs;}
}
__global__ void k_gemm(const float* A,const u16* W,float* C,int N,int K,int relu){
  __shared__ float As[16][17], Ws[16][17];
  int tx=threadIdx.x&15, ty=threadIdx.x>>4; int r=blockIdx.y*16+ty, c=blockIdx.x*16+tx; float acc=0;
  for(int k0=0;k0<K;k0+=16){ As[ty][tx]=A[(size_t)r*K+k0+tx]; Ws[ty][tx]=bf2f(W[(size_t)(k0+ty)*N+c]); __syncthreads();
    for(int k=0;k<16;k++) acc+=As[ty][k]*Ws[k][tx]; __syncthreads(); }
  if(relu) acc=fmaxf(acc,0.f); C[(size_t)r*N+c]=acc;
}
__global__ void k_phi(const float* qkv,const u16* wphi,float* phiq,float* phik){
  int idx=blockIdx.x*blockDim.x+threadIdx.x; if(idx>=S*H*2) return; int which=idx&1; int h=(idx>>1)%H; int s=(idx>>1)/H;
  const float* q=qkv+(size_t)s*3*D+which*D+h*DH; float p[DH];
  #pragma unroll
  for(int f=0;f<DH;f++){ float a=0;
    #pragma unroll
    for(int d=0;d<DH;d++) a+=q[d]*bf2f(wphi[h*DH*DH+d*DH+f]); p[f]=a; }
  float mx=-1e30f,mn=1e30f;
  #pragma unroll
  for(int f=0;f<DH;f++){mx=fmaxf(mx,p[f]);mn=fminf(mn,p[f]);}
  float s1=0,s2=0;
  #pragma unroll
  for(int f=0;f<DH;f++){s1+=__expf(p[f]-mx); s2+=__expf(mn-p[f]);}
  float* o=(which?phik:phiq)+((size_t)s*H+h)*40;
  #pragma unroll
  for(int f=0;f<DH;f++){o[f]=__expf(p[f]-mx)/s1; o[20+f]=__expf(mn-p[f])/s2;}
}
__global__ void k_kv(const float* phik,const float* qkv,float* kv){
  int h=blockIdx.x; int t=threadIdx.x; if(t>=840) return; int f=t/21,d=t%21; float a=0;
  for(int s=0;s<S;s++){ float v= d<20? qkv[(size_t)s*3*D+2*D+h*DH+d]:1.f; a+=phik[((size_t)s*H+h)*40+f]*v; }
  kv[h*840+t]=a;
}
__global__ void k_o(const float* phiq,const float* kv,float* o){
  int idx=blockIdx.x*blockDim.x+threadIdx.x; if(idx>=S*D) return; int s=idx/D,c=idx%D,h=c/DH,d=c%DH; float n=0,z=0;
  for(int f=0;f<40;f++){ float p=phiq[((size_t)s*H+h)*40+f]; n+=p*kv[h*840+f*21+d]; z+=p*kv[h*840+f*21+20]; }
  o[idx]=n/(z+1e-6f);
}
__global__ void k_add(const u16* x,const float* a,float* h){ int i=blockIdx.x*blockDim.x+threadIdx.x; if(i<S*D) h[i]=bf2f(x[i])+a[i]; }
__global__ void k_fin(const float* h,const float* a,u16* o){ int i=blockIdx.x*blockDim.x+threadIdx.x; if(i<S*D) o[i]=f2bf(h[i]+a[i]); }

extern "C" void kernel_launch(void* const* d_in, const int* in_sizes, int n_in, void* d_out, int out_size, void* d_ws, size_t ws_size, hipStream_t stream){
  const u16* x=(const u16*)d_in[0]; const u16* wqkv=(const u16*)d_in[1]; const u16* wphi=(const u16*)d_in[2]; const u16* wo=(const u16*)d_in[3];
  const u16* w1=(const u16*)d_in[5]; const u16* w2=(const u16*)d_in[6];
  float* ws=(float*)d_ws; float* xn=ws; float* qkv=xn+S*D; float* phiq=qkv+S*3*D; float* phik=phiq+S*H*40; float* kv=phik+S*H*40; float* o=kv+32*840+64;
  float* a=o+S*D; float* h=a+S*D; float* hn=h+S*D; float* hid=hn+S*D; float* m=hid+S*DFF;
  k_ln<<<S,256,0,stream>>>(x,nullptr,xn);
  k_gemm<<<dim3(3*D/16,S/16),256,0,stream>>>(xn,wqkv,qkv,3*D,D,0);
  k_phi<<<S*H*2/256,256,0,stream>>>(qkv,wphi,phiq,phik);
  k_kv<<<H,896,0,stream>>>(phik,qkv,kv);
  k_o<<<S*D/256,256,0,stream>>>(phiq,kv,o);
  k_gemm<<<dim3(D/16,S/16),256,0,stream>>>(o,wo,a,D,D,0);
  k_add<<<S*D/256,256,0,stream>>>(x,a,h);
  k_ln<<<S,256,0,stream>>>(nullptr,h,hn);
  k_gemm<<<dim3(DFF/16,S/16),256,0,stream>>>(hn,w1,hid,DFF,D,1);
  k_gemm<<<dim3(D/16,S/16),256,0,stream>>>(hid,w2,m,D,DFF,0);
  k_fin<<<S*D/256,256,0,stream>>>(h,m,(u16*)d_out);
}
```

```cpp
#include <hip/hip_runtime.h>
#include <hip/hip_bf16.h>

__global__ __launch_bounds__(256, 1) void fused_block(const void* x, const void* wqkv, const void* wphi, const void* wo,
                                                      const void* w1, const void* w2, void* out, void* ws) {
#if defined(__HIP_DEVICE_COMPILE__)
  __shared__ __attribute__((aligned(1024))) char lds[163840];
  asm volatile(
      "s_load_dwordx16 s[40:55], %0, 0\n"
      "s_mov_b32 s100, %1\n"
      "v_lshrrev_b32 v3, 6, %2\n"
      "v_and_b32 v1, 63, %2\n"
      "v_readfirstlane_b32 s101, v3\n"
      "s_and_b32 s97, s100, 7\n"
      "s_lshr_b32 s98, s97, 1\n"
      "s_bfe_u32 s96, s100, 0x20003\n"
      "s_lshl2_add_u32 s99, s98, s96\n"
      "s_and_b32 s94, s97, 1\n"
      "s_lshr_b32 s92, s100, 3\n"
      "s_lshr_b32 s93, s92, 2\n"
      "s_lshl3_add_u32 s95, s94, s93\n"
      "s_and_b32 s90, s100, 1\n"
      "s_lshl1_add_u32 s91, s99, s90\n"
      "s_lshr_b32 s89, s100, 5\n"
      "s_and_b32 s87, s92, 7\n"
      "s_lshl3_add_u32 s88, s94, s87\n"
      "s_lshr_b32 s85, s92, 3\n"
      "s_lshl2_add_u32 s86, s98, s85\n"
      "v_lshlrev_b32 v3, 4, v1\n"
      "s_mul_i32 s81, s87, 5\n"
      "s_lshr_b32 s82, s81, 2\n"
      "s_lshl1_add_u32 s83, s85, s82\n"
      "s_mul_i32 s80, s94, 5\n"
      "s_add_u32 s84, s83, s80\n"
      "s_mul_i32 s75, s84, 0xcd\n"
      "s_lshr_b32 s76, s75, 11\n"
      "s_mul_i32 s77, s76, 10\n"
      "s_sub_u32 s78, s84, s77\n"
      "s_lshl_b32 s79, s78, 7\n"
      "v_lshrrev_b32 v254, 4, v1\n"
      "v_and_b32 v253, 7, v1\n"
      "v_xor_b32 v255, v254, v253\n"
      "s_lshl_b32 s71, s88, 7\n"
      "s_lshl_b32 s70, s101, 5\n"
      "s_add_u32 s72, s71, s70\n"
      "s_mul_i32 s73, s72, 0x500\n"
      "v_lshrrev_b32 v250, 3, v1\n"
      "v_mul_u32_u24 v251, 0x500, v250\n"
      "v_add_u32 v252, s73, v251\n"
      "v_lshl_add_u32 v2, v255, 4, v252\n"
      "v_xor_b32 v247, 4, v255\n"
      "v_lshl_add_u32 v248, v247, 4, v252\n"
      "v_add_u32 v249, 0x2400, v248\n"
      "v_lshl_add_u32 v245, v255, 4, v252\n"
      "v_add_u32 v246, 0x4800, v245\n"
      "v_add_u32 v244, 0x6c00, v248\n"
      "s_mul_i32 s68, s101, 0xf000\n"
      "s_mul_i32 s66, s86, 0x50\n"
      "s_and_b32 s67, s66, 0xffc0\n"
      "s_add_u32 s69, s68, s67\n"
      "v_and_b32 v239, 1, v254\n"
      "v_lshlrev_b32 v240, 2, v239\n"
      "v_xor_b32 v241, v240, v253\n"
      "v_mul_u32_u24 v238, 0xf00, v250\n"
      "v_lshl_add_u32 v242, v241, 4, v238\n"
      "v_add_u32 v243, s69, v242\n"
      "v_add_u32 v237, 0x500, v243\n"
      "v_add_u32 v236, 0x7900, v243\n"
      "v_add_u32 v235, 0x1f0, v243\n"
      "v_add_u32 v234, 0x75f0, v243\n"
      "v_mov_b32 v233, v243\n"
      "v_add_u32 v232, 0x7400, v243\n"
      "s_mul_i32 s64, s86, 0x640\n"
      "s_lshl_b32 s63, s101, 9\n"
      "s_add_u32 s65, s64, s63\n"
      "v_add_u32 v231, s65, v3\n"
      "s_lshl_b32 s94, s101, 12\n"
      "s_add_u32 s98, s94, 0x1a000\n"
      "s_mul_i32 s90, s101, 0x1830\n"
      "s_lshl_b32 s85, s101, 9\n"
      "s_add_u32 s87, s85, 0x26800\n"
      "s_mul_i32 s85, s86, 0x50\n"
      "s_and_b32 s94, s85, 63\n"
      "v_and_b32 v251, 1, v1\n"
      "v_mul_u32_u24 v230, 0x48, v251\n"
      "v_add_u32 v245, s94, v230\n"
      "v_bfe_u32 v238, v1, 2, 2\n"
      "v_lshrrev_b32 v239, 1, v238\n"
      "v_lshlrev_b32 v250, 6, v239\n"
      "v_lshrrev_b32 v254, 4, v1\n"
      "v_lshrrev_b32 v255, 1, v254\n"
      "v_lshl_add_u32 v228, v255, 3, v238\n"
      "v_and_b32 v243, 1, v254\n"
      "v_bfe_u32 v242, v1, 1, 1\n"
      "v_lshl_add_u32 v252, v243, 1, v242\n"
      "v_mul_u32_u24 v253, 0x810, v252\n"
      "v_lshl_add_u32 v229, v228, 7, v253\n"
      "v_xad_u32 v247, v245, v250, v229\n"
      "v_mov_b32 v241, 0x800\n"
      "v_cmp_gt_u32 vcc, 3, v252\n"
      "s_nop 1\n"
      "v_cndmask_b32 v248, v241, v247, vcc\n"
      "v_add_u32 v227, 0x200, v247\n"
      "v_cmp_gt_u32 vcc, 3, v252\n"
      "s_nop 1\n"
      "v_cndmask_b32 v240, v241, v227, vcc\n"
      "v_lshl_add_u32 v223, v251, 2, v252\n"
      "s_add_u32 s78, s94, 8\n"
      "v_lshl_add_u32 v224, v223, 3, s78\n"
      "v_xor_b32 v225, v250, v224\n"
      "v_lshl_add_u32 v226, v228, 7, v225\n"
      "s_lshl_b32 s97, s101, 12\n"
      "s_add_u32 s75, s97, 0x1a000\n"
      "v_and_b32 v220, 31, v1\n"
      "v_bfe_u32 v217, v1, 1, 3\n"
      "s_and_b32 s93, s101, 3\n"
      "s_lshl_b32 s96, s93, 1\n"
      "v_lshrrev_b32 v215, 5, v1\n"
      "v_add_u32 v216, s96, v215\n"
      "v_xor_b32 v218, v217, v216\n"
      "v_lshlrev_b32 v219, 4, v218\n"
      "v_lshl_add_u32 v221, v220, 7, v219\n"
      "v_add_u32 v222, s75, v221\n"
      "s_mul_i32 s92, s93, 0x1830\n"
      "v_add_u32 v214, s92, v226\n"
      "v_add_u32 v213, s92, v248\n"
      "v_add_u32 v212, s92, v240\n"
      "s_add_u32 s73, s101, 1\n"
      "s_and_b32 s76, s73, 3\n"
      "s_lshl_b32 s77, s76, 1\n"
      "v_add_u32 v207, s77, v215\n"
      "v_xor_b32 v208, v217, v207\n"
      "v_lshlrev_b32 v209, 4, v208\n"
      "v_lshl_add_u32 v210, v220, 7, v209\n"
      "v_add_u32 v211, s75, v210\n"
      "s_mul_i32 s72, s76, 0x1830\n"
      "v_add_u32 v206, s72, v226\n"
      "v_add_u32 v205, s72, v248\n"
      "v_add_u32 v204, s72, v240\n"
      "s_add_u32 s81, s101, 2\n"
      "s_and_b32 s82, s81, 3\n"
      "s_lshl_b32 s83, s82, 1\n"
      "v_add_u32 v199, s83, v215\n"
      "s_mov_b32 s6, 0x280000\n"
      "s_mov_b32 s7, 0x20000\n"
      "s_mov_b32 s10, 0x258000\n"
      "s_mov_b32 s11, 0x20000\n"
      "s_mov_b32 s14, 0x6400\n"
      "s_mov_b32 s15, 0x20000\n"
      "s_mov_b32 s18, 0x21c000\n"
      "s_mov_b32 s19, 0x20000\n"
      "s_mov_b32 s22, 0x21c000\n"
      "s_mov_b32 s23, 0x20000\n"
      "s_mov_b32 s30, 0x280000\n"
      "s_mov_b32 s31, 0x20000\n"
      "s_mov_b32 s38, 0xf2d000\n"
      "s_mov_b32 s39, 0x20000\n"
      "s_waitcnt lgkmcnt(0)\n"
      "s_mov_b64 s[8:9], s[42:43]\n"
      "s_mov_b64 s[4:5], s[40:41]\n"
      "s_mul_i32 s74, s79, 1920\n"
      "s_add_u32 m0, s90, 0\n"
      "s_nop 0\n"
      "buffer_load_dwordx4 v237, s[8:11], s74 offen offset:0 lds\n"
      "buffer_load_dwordx4 v236, s[8:11], s74 offen offset:1024 lds\n"
      "buffer_load_dwordx4 v235, s[8:11], s74 offen offset:2064 lds\n"
      "buffer_load_dwordx4 v234, s[8:11], s74 offen offset:3088 lds\n"
      "s_add_u32 m0, s90, 4128\n"
      "s_nop 0\n"
      "buffer_load_dwordx4 v233, s[8:11], s74 offen offset:0 lds\n"
      "buffer_load_dwordx4 v232, s[8:11], s74 offen offset:1024 lds\n"
      "s_add_u32 m0, s98, 0\n"
      "s_nop 0\n"
      "buffer_load_dwordx4 v2, s[4:7], s79 offen offset:0 lds\n"
      "buffer_load_dwordx4 v249, s[4:7], s79 offen offset:1024 lds\n"
      "s_mov_b64 s[12:13], s[44:45]\n"
      "s_mov_b64 s[20:21], s[48:49]\n"
      "s_mov_b64 s[16:17], s[50:51]\n"
      "s_mov_b64 s[28:29], s[52:53]\n"
      "s_mov_b64 s[36:37], s[54:55]\n"
      "s_mov_b64 s[34:35], s[46:47]\n"
      "v_xor_b32 v200, v217, v199\n"
      "v_lshlrev_b32 v201, 4, v200\n"
      "v_lshl_add_u32 v202, v220, 7, v201\n"
      "v_add_u32 v203, s75, v202\n"
      "s_mul_i32 s80, s82, 0x1830\n"
      "v_add_u32 v198, s80, v226\n"
      "v_add_u32 v197, s80, v248\n"
      "v_add_u32 v196, s80, v240\n"
      "s_add_u32 s69, s101, 3\n"
      "s_and_b32 s70, s69, 3\n"
      "s_lshl_b32 s71, s70, 1\n"
      "v_add_u32 v191, s71, v215\n"
      "v_xor_b32 v192, v217, v191\n"
      "v_lshlrev_b32 v193, 4, v192\n"
      "v_lshl_add_u32 v194, v220, 7, v193\n"
      "v_add_u32 v195, s75, v194\n"
      "s_mul_i32 s68, s70, 0x1830\n"
      "v_add_u32 v190, s68, v226\n"
      "v_add_u32 v189, s68, v248\n"
      "v_add_u32 v188, s68, v240\n"
      "v_mul_u32_u24 v183, 0xa0, v251\n"
      "v_sub_u32 v184, 0xa0, v183\n"
      "v_mul_u32_u24 v180, 0x320, v251\n"
      "v_mad_u32_u24 v181, v238, 40, v180\n"
      "v_add_u32 v182, 0x26800, v181\n"
      "v_add_u32 v185, v184, v182\n"
      "v_lshl_add_u32 v186, v252, 3, v185\n"
      "v_mov_b32 v179, 0x27000\n"
      "v_cmp_eq_u32 vcc, v251, v215\n"
      "s_nop 1\n"
      "v_cndmask_b32 v187, v179, v186, vcc\n"
      "v_mul_u32_u24 v175, 0x280, v251\n"
      "v_add_u32 v176, v175, v182\n"
      "v_lshl_add_u32 v177, v252, 3, v176\n"
      "v_cmp_eq_u32 vcc, v251, v215\n"
      "s_nop 1\n"
      "v_cndmask_b32 v178, v179, v177, vcc\n"
      "v_add_u32 v173, 32, v185\n"
      "v_lshl_add_u32 v172, v252, 1, v251\n"
      "v_cmp_eq_u32 vcc, v172, v215\n"
      "s_nop 1\n"
      "v_cndmask_b32 v174, v179, v173, vcc\n"
      "v_add_u32 v170, 32, v176\n"
      "v_cmp_eq_u32 vcc, v172, v215\n"
      "s_nop 1\n"
      "v_cndmask_b32 v171, v179, v170, vcc\n"
      "s_mul_i32 s85, s101, 0x1400\n"
      "s_mul_i32 s78, s101, 0xa000\n"
      "s_mul_i32 s97, s101, 0x1400\n"
      "s_add_u32 s75, s97, 0xf000\n"
      "v_and_b32 v245, 3, v1\n"
      "v_lshrrev_b32 v223, 2, v1\n"
      "v_mul_u32_u24 v230, 0xa0, v223\n"
      "v_lshl_add_u32 v247, v245, 3, v230\n"
      "v_add_u32 v248, s75, v247\n"
      "s_mul_i32 s97, s89, 0xa0\n"
      "s_and_b32 s93, s89, 1\n"
      "s_lshl_b32 s96, s93, 5\n"
      "s_add_u32 s75, s97, s96\n"
      "v_mov_b32 v247, s75\n"
      "s_add_u32 s76, s97, 0x80\n"
      "s_lshl_b32 s73, s93, 7\n"
      "s_sub_u32 s77, s76, s73\n"
      "s_add_u32 s92, s77, 0xffffff80\n"
      "v_mov_b32 v245, s92\n"
      "v_mul_u32_u24 v223, 0xcd, v1\n"
      "v_lshrrev_b32 v230, 11, v223\n"
      "v_mul_u32_u24 v210, 10, v230\n"
      "v_sub_u32 v215, v1, v210\n"
      "v_cmp_gt_u32 vcc, 8, v215\n"
      "s_nop 1\n"
      "v_cndmask_b32 v199, v245, v247, vcc\n"
      "v_lshl_add_u32 v202, v215, 4, v199\n"
      "v_and_b32 v186, 3, v230\n"
      "v_bfe_u32 v175, v230, 2, 2\n"
      "v_lshrrev_b32 v251, 4, v230\n"
      "v_lshlrev_b32 v170, 2, v251\n"
      "v_lshl_add_u32 v179, v175, 3, v170\n"
      "v_add_u32 v191, v186, v179\n"
      "v_mul_u32_u24 v194, 0x500, v191\n"
      "v_add_u32 v207, v202, v194\n"
      "v_add_u32 v215, 64, v1\n"
      "v_mul_u32_u24 v223, 0xcd, v215\n"
      "v_lshrrev_b32 v230, 11, v223\n"
      "v_mul_u32_u24 v202, 10, v230\n"
      "v_sub_u32 v210, v215, v202\n"
      "v_cmp_gt_u32 vcc, 8, v210\n"
      "s_nop 1\n"
      "v_cndmask_b32 v186, v245, v247, vcc\n"
      "v_lshl_add_u32 v191, v210, 4, v186\n"
      "v_and_b32 v170, 3, v230\n"
      "v_bfe_u32 v250, v230, 2, 2\n"
      "v_lshrrev_b32 v238, 4, v230\n"
      "v_lshlrev_b32 v239, 2, v238\n"
      "v_lshl_add_u32 v251, v250, 3, v239\n"
      "v_add_u32 v175, v170, v251\n"
      "v_mul_u32_u24 v179, 0x500, v175\n"
      "v_add_u32 v194, v191, v179\n"
      "v_add_u32 v199, 0xfffffc00, v194\n"
      "v_add_u32 v215, 0x80, v1\n"
      "v_mul_u32_u24 v223, 0xcd, v215\n"
      "v_lshrrev_b32 v230, 11, v223\n"
      "v_mul_u32_u24 v202, 10, v230\n"
      "v_sub_u32 v210, v215, v202\n"
      "v_cmp_gt_u32 vcc, 8, v210\n"
      "s_nop 1\n"
      "v_cndmask_b32 v179, v245, v247, vcc\n"
      "v_lshl_add_u32 v186, v210, 4, v179\n"
      "v_and_b32 v251, 3, v230\n"
      "v_bfe_u32 v239, v230, 2, 2\n"
      "v_lshrrev_b32 v229, 4, v230\n"
      "v_lshlrev_b32 v238, 2, v229\n"
      "v_lshl_add_u32 v250, v239, 3, v238\n"
      "v_add_u32 v170, v251, v250\n"
      "v_mul_u32_u24 v175, 0x500, v170\n"
      "v_add_u32 v191, v186, v175\n"
      "v_add_u32 v194, 0xfffff800, v191\n"
      "v_add_u32 v215, 0xc0, v1\n"
      "v_mul_u32_u24 v223, 0xcd, v215\n"
      "v_lshrrev_b32 v230, 11, v223\n"
      "v_mul_u32_u24 v202, 10, v230\n"
      "v_sub_u32 v210, v215, v202\n"
      "v_cmp_gt_u32 vcc, 8, v210\n"
      "s_nop 1\n"
      "v_cndmask_b32 v175, v245, v247, vcc\n"
      "v_lshl_add_u32 v179, v210, 4, v175\n"
      "v_and_b32 v250, 3, v230\n"
      "v_bfe_u32 v238, v230, 2, 2\n"
      "v_lshrrev_b32 v228, 4, v230\n"
      "v_lshlrev_b32 v229, 2, v228\n"
      "v_lshl_add_u32 v239, v238, 3, v229\n"
      "v_add_u32 v251, v250, v239\n"
      "v_mul_u32_u24 v170, 0x500, v251\n"
      "v_add_u32 v186, v179, v170\n"
      "v_add_u32 v191, 0xfffff400, v186\n"
      "v_add_u32 v215, 0x100, v1\n"
      "v_mul_u32_u24 v223, 0xcd, v215\n"
      "v_lshrrev_b32 v230, 11, v223\n"
      "v_mul_u32_u24 v202, 10, v230\n"
      "v_sub_u32 v210, v215, v202\n"
      "v_cmp_gt_u32 vcc, 8, v210\n"
      "s_nop 1\n"
      "v_cndmask_b32 v175, v245, v247, vcc\n"
      "v_lshl_add_u32 v179, v210, 4, v175\n"
      "v_and_b32 v250, 3, v230\n"
      "v_bfe_u32 v238, v230, 2, 2\n"
      "v_lshrrev_b32 v228, 4, v230\n"
      "v_lshlrev_b32 v229, 2, v228\n"
      "v_lshl_add_u32 v239, v238, 3, v229\n"
      "v_add_u32 v251, v250, v239\n"
      "v_mul_u32_u24 v170, 0x500, v251\n"
      "v_add_u32 v186, v179, v170\n"
      "s_lshr_b32 s96, s100, 3\n"
      "s_bfe_u32 s97, s96, 0x10003\n"
      "s_and_b32 s92, s100, 7\n"
      "s_bfe_u32 s93, s92, 0x10001\n"
      "s_lshr_b32 s72, s96, 4\n"
      "s_and_b32 s83, s96, 7\n"
      "s_lshl3_add_u32 s73, s72, s83\n"
      "s_lshl3_add_u32 s76, s73, s92\n"
      "s_lshl2_add_u32 s77, s76, s101\n"
      "s_cmp_eq_u32 s97, s93\n"
      "s_cselect_b32 s75, s77, 0x4000\n"
      "s_mul_i32 s82, s75, 0x700\n"
      "v_lshl_add_u32 v245, v1, 7, s82\n"
      "v_mov_b32 v230, 0x40000000\n"
      "v_cmp_gt_u32 vcc, 14, v1\n"
      "s_nop 1\n"
      "v_cndmask_b32 v247, v230, v245, vcc\n"
      "s_and_b32 s71, s92, 1\n"
      "s_mul_i32 s80, s71, 0x700\n"
      "s_lshl2_add_u32 s68, s96, s101\n"
      "s_mul_i32 s69, s68, 0x1b00\n"
      "s_add_u32 s70, s69, 0x144000\n"
      "s_add_u32 s81, s80, s70\n"
      "v_mul_u32_u24 v175, 0x124a, v1\n"
      "v_lshrrev_b32 v179, 16, v175\n"
      "v_mul_u32_u24 v202, 0x680, v179\n"
      "v_lshl_add_u32 v210, v1, 7, v202\n"
      "v_add_u32 v215, s81, v210\n"
      "v_cmp_gt_u32 vcc, 28, v1\n"
      "s_nop 1\n"
      "v_cndmask_b32 v223, v230, v215, vcc\n"
      "s_mul_i32 s67, s75, 0x1100\n"
      "v_lshl_add_u32 v251, v1, 7, s67\n"
      "v_cmp_gt_u32 vcc, 34, v1\n"
      "s_nop 1\n"
      "v_cndmask_b32 v170, v230, v251, vcc\n"
      "s_lshl_b32 s65, s75, 9\n"
      "s_add_u32 s66, s65, 0x1000\n"
      "v_lshl_add_u32 v239, v1, 7, s66\n"
      "s_cmp_lt_u32 s75, 0x180\n"
      "s_cselect_b32 s64, 0, 64\n"
      "v_add_u32 v238, s64, v1\n"
      "v_cmp_gt_u32 vcc, 4, v238\n"
      "s_nop 1\n"
      "v_cndmask_b32 v250, v230, v239, vcc\n"
      "s_mul_i32 s97, s75, 0x1400\n"
      "v_lshl_add_u32 v228, v1, 7, s97\n"
      "v_cmp_gt_u32 vcc, 40, v1\n"
      "s_nop 1\n"
      "v_cndmask_b32 v229, v230, v228, vcc\n"
      "s_lshr_b32 s96, s100, 3\n"
      "s_bfe_u32 s97, s96, 0x10003\n"
      "s_and_b32 s92, s100, 7\n"
      "s_bfe_u32 s93, s92, 0x10001\n"
      "s_lshr_b32 s73, s96, 4\n"
      "s_and_b32 s72, s96, 7\n"
      "s_lshl3_add_u32 s76, s73, s72\n"
      "s_lshl3_add_u32 s77, s76, s92\n"
      "s_cmp_eq_u32 s97, s93\n"
      "s_cselect_b32 s75, s77, 0xff\n"
      "s_and_b32 s82, s75, 1\n"
      "s_lshl2_add_u32 s83, s82, s101\n"
      "s_cmp_lt_u32 s75, 108\n"
      "s_cselect_b32 s18, s18, 0\n"
      "s_lshr_b32 s80, s75, 1\n"
      "s_mul_i32 s81, s80, 0xa000\n"
      "s_mul_i32 s68, s83, 0x43800\n"
      "s_mul_i32 s67, s80, 0x1400\n"
      "s_add_u32 s69, s68, s67\n"
      "s_add_u32 s70, s69, 0xd11000\n"
      "s_cmp_lt_u32 s75, 0x6c\n"
      "s_cselect_b32 s71, s70, 0x40000000\n"
      "s_add_u32 s66, s71, 0x1000\n"
      "s_mul_i32 s59, s80, 0x124a\n"
      "s_lshr_b32 s60, s59, 16\n"
      "s_mul_i32 s61, s60, 14\n"
      "s_sub_u32 s62, s80, s61\n"
      "s_lshl2_add_u32 s57, s83, s60\n"
      "s_lshl_b32 s58, s57, 10\n"
      "s_lshl3_add_u32 s63, s62, s58\n"
      "s_add_u32 s64, s63, 0x29000\n"
      "s_cmp_lt_u32 s75, 0x6c\n"
      "s_cselect_b32 s65, s64, 0x40000000\n"
      "v_lshl_add_u32 v245, v1, 7, s65\n"
      "v_mov_b32 v230, 0x40000000\n"
      "v_cmp_gt_u32 vcc, 8, v1\n"
      "s_nop 1\n"
      "v_cndmask_b32 v251, v230, v245, vcc\n"
      "s_lshl2_add_u32 s67, s89, s101\n"
      "s_lshl_b32 s70, s67, 10\n"
      "s_lshr_b32 s96, s91, 2\n"
      "s_lshl_b32 s97, s96, 7\n"
      "s_add_u32 s80, s70, s97\n"
      "s_add_u32 s82, s80, 0x29000\n"
      "v_lshl_add_u32 v228, v1, 3, s82\n"
      "s_mov_b32 s93, 0\n"
      "s_mov_b32 s77, 0xfff\n"
      "s_cmp_lt_u32 s101, 3\n"
      "s_cselect_b32 s92, 0x3fff, s77\n"
      "s_lshr_b32 s69, s75, 1\n"
      "s_mul_i32 s64, s69, 0x2493\n"
      "s_lshr_b32 s65, s64, 16\n"
      "s_mul_i32 s68, s65, 7\n"
      "s_sub_u32 s72, s69, s68\n"
      "s_cmp_ge_u32 s69, 42\n"
      "s_cselect_b32 s63, 8, 0\n"
      "s_add_u32 s73, s72, s63\n"
      "s_cmp_eq_u32 s73, 3\n"
      "s_cselect_b32 s76, 24, 0\n"
      "s_mul_i32 s80, s83, 0xa0\n"
      "s_and_b32 s70, s101, 1\n"
      "s_lshl_b32 s77, s70, 5\n"
      "s_add_u32 s82, s80, s77\n"
      "v_mov_b32 v245, s82\n"
      "s_add_u32 s96, s80, 0x80\n"
      "s_lshl_b32 s73, s70, 7\n"
      "s_sub_u32 s97, s96, s73\n"
      "s_add_u32 s67, s97, 0xffffff80\n"
      "v_mov_b32 v230, s67\n"
      "v_mul_u32_u24 v210, 0xcd, v1\n"
      "v_lshrrev_b32 v215, 11, v210\n"
      "v_mul_u32_u24 v179, 10, v215\n"
      "v_sub_u32 v202, v1, v179\n"
      "v_cmp_gt_u32 vcc, 8, v202\n"
      "s_nop 1\n"
      "v_cndmask_b32 v238, v230, v245, vcc\n"
      "v_lshl_add_u32 v239, v202, 4, v238\n"
      "v_lshrrev_b32 v200, 3, v215\n"
      "v_lshrrev_b32 v193, 4, v215\n"
      "v_xor_b32 v201, v200, v193\n"
      "v_and_b32 v208, 1, v201\n"
      "v_mul_u32_u24 v209, s76, v208\n"
      "v_xor_b32 v220, v209, v215\n"
      "v_mul_u32_u24 v221, 0x500, v220\n"
      "v_add_u32 v175, v239, v221\n"
      "v_add_u32 v202, 64, v1\n"
      "v_mul_u32_u24 v210, 0xcd, v202\n"
      "v_lshrrev_b32 v215, 11, v210\n"
      "v_mul_u32_u24 v239, 10, v215\n"
      "v_sub_u32 v179, v202, v239\n"
      "v_cmp_gt_u32 vcc, 8, v179\n"
      "s_nop 1\n"
      "v_cndmask_b32 v209, v230, v245, vcc\n"
      "v_lshl_add_u32 v220, v179, 4, v209\n"
      "v_lshrrev_b32 v185, 3, v215\n"
      "v_lshrrev_b32 v184, 4, v215\n"
      "v_xor_b32 v192, v185, v184\n"
      "v_and_b32 v193, 1, v192\n"
      "v_mul_u32_u24 v200, s76, v193\n"
      "v_xor_b32 v201, v200, v215\n"
      "v_mul_u32_u24 v208, 0x500, v201\n"
      "v_add_u32 v221, v220, v208\n"
      "v_add_u32 v238, 0xfffffc00, v221\n"
      "v_add_u32 v210, 0x80, v1\n"
      "v_mul_u32_u24 v215, 0xcd, v210\n"
      "v_lshrrev_b32 v239, 11, v215\n"
      "v_mul_u32_u24 v179, 10, v239\n"
      "v_sub_u32 v202, v210, v179\n"
      "v_cmp_gt_u32 vcc, 8, v202\n"
      "s_nop 1\n"
      "v_cndmask_b32 v208, v230, v245, vcc\n"
      "v_lshl_add_u32 v209, v202, 4, v208\n"
      "v_lshrrev_b32 v184, 3, v239\n"
      "v_lshrrev_b32 v177, 4, v239\n"
      "v_xor_b32 v185, v184, v177\n"
      "v_and_b32 v192, 1, v185\n"
      "v_mul_u32_u24 v193, s76, v192\n"
      "v_xor_b32 v200, v193, v239\n"
      "v_mul_u32_u24 v201, 0x500, v200\n"
      "v_add_u32 v220, v209, v201\n"
      "v_add_u32 v221, 0xfffff800, v220\n"
      "v_add_u32 v215, 0xc0, v1\n"
      "v_mul_u32_u24 v220, 0xcd, v215\n"
      "v_lshrrev_b32 v239, 11, v220\n"
      "v_mul_u32_u24 v202, 10, v239\n"
      "v_sub_u32 v210, v215, v202\n"
      "v_cmp_gt_u32 vcc, 8, v210\n"
      "s_nop 1\n"
      "v_cndmask_b32 v201, v230, v245, vcc\n"
      "v_lshl_add_u32 v208, v210, 4, v201\n"
      "v_lshrrev_b32 v177, 3, v239\n"
      "v_lshrrev_b32 v176, 4, v239\n"
      "v_xor_b32 v184, v177, v176\n"
      "v_and_b32 v185, 1, v184\n"
      "v_mul_u32_u24 v192, s76, v185\n"
      "v_xor_b32 v193, v192, v239\n"
      "v_mul_u32_u24 v200, 0x500, v193\n"
      "v_add_u32 v209, v208, v200\n"
      "v_add_u32 v179, 0xfffff400, v209\n"
      "v_add_u32 v215, 0x100, v1\n"
      "v_mul_u32_u24 v220, 0xcd, v215\n"
      "v_lshrrev_b32 v239, 11, v220\n"
      "v_mul_u32_u24 v202, 10, v239\n"
      "v_sub_u32 v210, v215, v202\n"
      "v_cmp_gt_u32 vcc, 8, v210\n"
      "s_nop 1\n"
      "v_cndmask_b32 v201, v230, v245, vcc\n"
      "v_lshl_add_u32 v208, v210, 4, v201\n"
      "buffer_load_dwordx4 v246, s[4:7], s79 offen offset:2048 lds\n"
      "buffer_load_dwordx4 v244, s[4:7], s79 offen offset:3072 lds\n"
      "s_add_u32 s79, s79, 128\n"
      "s_cmp_eq_u32 s79, 1280\n"
      "s_cselect_b32 s79, 0, s79\n"
      "s_mov_b32 m0, s87\n"
      "s_mov_b32 exec_hi, 0\n"
      "buffer_load_dwordx4 v231, s[12:15], 0 offen lds\n"
      "s_mov_b32 exec_hi, -1\n"
      "s_mov_b64 s[12:13], s[34:35]\n"
      "s_mov_b32 s14, 819200\n"
      "buffer_load_dwordx2 v[166:167], off, s[36:39], 0 sc1\n"
      "s_getpc_b64 s[52:53]\n"
      "Lpc_%=:\n"
      "s_mov_b32 s54, Lend_%=-Lpc_%=\n"
      "s_mov_b32 s55, 0x20000\n"
      "s_lshr_b32 s35, s100, 3\n"
      "s_lshl_b32 s56, s35, 5\n"
      "v_add_u32 v163, s56, v1\n"
      "v_lshlrev_b32 v164, 6, v163\n"
      "v_mov_b32 v162, 0x40000000\n"
      "s_lshl_b32 s34, s101, 6\n"
      "v_add_u32 v161, s34, v1\n"
      "v_cmp_gt_u32 vcc, 32, v161\n"
      "s_nop 1\n"
      "v_cndmask_b32 v165, v162, v164, vcc\n"
      "buffer_load_dword v165, v165, s[52:55], 0 offen\n"
      "s_mul_i32 s74, s79, 1920\n"
      "s_add_u32 m0, s90, 24768\n"
      "s_nop 0\n"
      "buffer_load_dwordx4 v237, s[8:11], s74 offen offset:0 lds\n"
      "v_lshrrev_b32 v177, 3, v239\n"
      "v_lshrrev_b32 v176, 4, v239\n"
      "v_xor_b32 v184, v177, v176\n"
      "v_and_b32 v185, 1, v184\n"
      "v_mul_u32_u24 v192, s76, v185\n"
      "v_xor_b32 v193, v192, v239\n"
      "v_mul_u32_u24 v200, 0x500, v193\n"
      "v_add_u32 v209, v208, v200\n"
      "s_lshl_b32 s67, s91, 6\n"
      "s_lshl4_add_u32 s70, s101, s67\n"
      "s_mul_i32 s75, s70, 0x500\n"
      "s_mul_i32 s96, s89, 0xa0\n"
      "s_and_b32 s97, s96, 0xffc0\n"
      "s_add_u32 s80, s75, s97\n"
      "v_mul_u32_u24 v215, 0xab, v1\n"
      "v_lshrrev_b32 v220, 11, v215\n"
      "v_mul_u32_u24 v230, 0x440, v220\n"
      "v_lshl_add_u32 v239, v1, 4, v230\n"
      "v_add_u32 v245, s80, v239\n"
      "s_add_u32 s83, s80, 0xfffffc00\n"
      "buffer_load_dwordx4 v236, s[8:11], s74 offen offset:1024 lds\n"
      "v_add_u32 v202, 64, v1\n"
      "v_mul_u32_u24 v193, 0xab, v202\n"
      "v_lshrrev_b32 v200, 11, v193\n"
      "v_mul_u32_u24 v201, 0x440, v200\n"
      "v_lshl_add_u32 v208, v202, 4, v201\n"
      "v_add_u32 v210, s83, v208\n"
      "s_add_u32 s82, s80, 0xfffff800\n"
      "v_add_u32 v184, 0x80, v1\n"
      "v_mul_u32_u24 v173, 0xab, v184\n"
      "v_lshrrev_b32 v176, 11, v173\n"
      "v_mul_u32_u24 v177, 0x440, v176\n"
      "v_lshl_add_u32 v185, v184, 4, v177\n"
      "v_add_u32 v192, s82, v185\n"
      "s_mul_i32 s77, s101, 0xc00\n"
      "s_mul_i32 s75, s89, 0x43800\n"
      "s_mul_i32 s70, s101, 0x11800\n"
      "s_add_u32 s76, s75, s70\n"
      "s_add_u32 s80, s76, 0xd11000\n"
      "v_add_u32 v239, s80, v3\n"
      "s_cmp_lt_u32 s101, 3\n"
      "s_cselect_b32 s67, 0, 0x4000000\n"
      "buffer_load_dwordx4 v235, s[8:11], s74 offen offset:2064 lds\n"
      "s_and_b32 s96, s101, 1\n"
      "s_lshl_b32 s97, s96, 11\n"
      "s_lshl_b32 s82, s96, 11\n"
      "s_sub_u32 s83, 0x800, s82\n"
      "s_add_u32 s73, s97, s67\n"
      "s_add_u32 s72, s83, s67\n"
      "s_mul_i32 s80, s101, 0x1c00\n"
      "s_add_u32 s82, s80, 0x5000\n"
      "s_mul_i32 s76, s101, 0x1b000\n"
      "s_add_u32 s75, s80, 0x1a000\n"
      "v_and_b32 v215, 3, v1\n"
      "v_lshrrev_b32 v202, 2, v1\n"
      "v_mul_u32_u24 v208, 0xe0, v202\n"
      "v_lshl_add_u32 v220, v215, 3, v208\n"
      "v_add_u32 v230, s75, v220\n"
      "v_add_u32 v193, s75, v3\n"
      "s_mul_i32 s69, s95, 7\n"
      "s_and_b32 s68, s95, 1\n"
      "s_add_u32 s70, s69, s68\n"
      "s_mul_i32 s64, s95, 6\n"
      "buffer_load_dwordx4 v234, s[8:11], s74 offen offset:3088 lds\n"
      "s_add_u32 s65, s64, 12\n"
      "s_cmp_lt_u32 s95, 12\n"
      "s_cselect_b32 s75, s70, s65\n"
      "s_lshl_b32 s80, s75, 5\n"
      "v_mov_b32 v220, s80\n"
      "s_mul_i32 s65, s95, 7\n"
      "s_add_u32 s68, s65, 6\n"
      "s_and_b32 s63, s95, 1\n"
      "s_mul_i32 s64, s63, 6\n"
      "s_sub_u32 s69, s68, s64\n"
      "s_lshl_b32 s70, s69, 5\n"
      "s_cmp_lt_u32 s95, 12\n"
      "s_cselect_b32 s75, s70, 0x40000000\n"
      "s_add_u32 s80, s75, 0xffffff40\n"
      "v_mov_b32 v215, s80\n"
      "v_mul_u32_u24 v202, 0x124a, v1\n"
      "v_lshrrev_b32 v208, 16, v202\n"
      "v_mul_u32_u24 v200, 14, v208\n"
      "v_sub_u32 v201, v1, v200\n"
      "v_cmp_gt_u32 vcc, 12, v201\n"
      "s_nop 1\n"
      "v_cndmask_b32 v177, v215, v220, vcc\n"
      "s_add_u32 m0, s90, 28896\n"
      "s_nop 0\n"
      "buffer_load_dwordx4 v233, s[8:11], s74 offen offset:0 lds\n"
      "v_lshl_add_u32 v184, v201, 4, v177\n"
      "v_mul_u32_u24 v176, 0xd80, v208\n"
      "v_add_u32 v185, v184, v176\n"
      "v_add_u32 v184, 64, v1\n"
      "v_mul_u32_u24 v202, 0x124a, v184\n"
      "v_lshrrev_b32 v208, 16, v202\n"
      "v_mul_u32_u24 v200, 14, v208\n"
      "v_sub_u32 v201, v184, v200\n"
      "v_cmp_gt_u32 vcc, 12, v201\n"
      "s_nop 1\n"
      "v_cndmask_b32 v172, v215, v220, vcc\n"
      "v_lshl_add_u32 v173, v201, 4, v172\n"
      "v_mul_u32_u24 v169, 0xd80, v208\n"
      "v_add_u32 v176, v173, v169\n"
      "v_add_u32 v177, 0xfffffc00, v176\n"
      "v_add_u32 v184, 0x80, v1\n"
      "v_mul_u32_u24 v202, 0x124a, v184\n"
      "v_lshrrev_b32 v208, 16, v202\n"
      "v_mul_u32_u24 v201, 14, v208\n"
      "v_sub_u32 v176, v184, v201\n"
      "v_cmp_gt_u32 vcc, 12, v176\n"
      "s_nop 1\n"
      "v_cndmask_b32 v169, v215, v220, vcc\n"
      "buffer_load_dwordx4 v232, s[8:11], s74 offen offset:1024 lds\n"
      "v_lshl_add_u32 v172, v176, 4, v169\n"
      "v_mul_u32_u24 v168, 0xd80, v208\n"
      "v_add_u32 v173, v172, v168\n"
      "v_add_u32 v200, 0xfffff800, v173\n"
      "v_add_u32 v201, 0xc0, v1\n"
      "v_mul_u32_u24 v202, 0x124a, v201\n"
      "v_lshrrev_b32 v208, 16, v202\n"
      "v_mul_u32_u24 v176, 14, v208\n"
      "v_sub_u32 v184, v201, v176\n"
      "v_cmp_gt_u32 vcc, 12, v184\n"
      "s_nop 1\n"
      "v_cndmask_b32 v168, v215, v220, vcc\n"
      "v_lshl_add_u32 v169, v184, 4, v168\n"
      "v_mul_u32_u24 v255, 0xd80, v208\n"
      "v_add_u32 v172, v169, v255\n"
      "v_add_u32 v173, 0xfffff400, v172\n"
      "v_add_u32 v201, 0x100, v1\n"
      "v_mul_u32_u24 v202, 0x124a, v201\n"
      "v_lshrrev_b32 v208, 16, v202\n"
      "v_mul_u32_u24 v176, 14, v208\n"
      "v_sub_u32 v184, v201, v176\n"
      "s_add_u32 m0, s98, 16384\n"
      "s_nop 0\n"
      "buffer_load_dwordx4 v2, s[4:7], s79 offen offset:0 lds\n"
      "v_cmp_gt_u32 vcc, 12, v184\n"
      "s_nop 1\n"
      "v_cndmask_b32 v168, v215, v220, vcc\n"
      "v_lshl_add_u32 v169, v184, 4, v168\n"
      "v_mul_u32_u24 v255, 0xd80, v208\n"
      "v_add_u32 v172, v169, v255\n"
      "v_add_u32 v201, 0x140, v1\n"
      "v_mul_u32_u24 v202, 0x124a, v201\n"
      "v_lshrrev_b32 v208, 16, v202\n"
      "v_mul_u32_u24 v176, 14, v208\n"
      "v_sub_u32 v184, v201, v176\n"
      "v_cmp_gt_u32 vcc, 12, v184\n"
      "s_nop 1\n"
      "v_cndmask_b32 v254, v215, v220, vcc\n"
      "v_lshl_add_u32 v255, v184, 4, v254\n"
      "v_mul_u32_u24 v253, 0xd80, v208\n"
      "v_add_u32 v168, v255, v253\n"
      "v_add_u32 v169, 0xfffffc00, v168\n"
      "v_add_u32 v201, 0x180, v1\n"
      "v_mul_u32_u24 v202, 0x124a, v201\n"
      "v_lshrrev_b32 v208, 16, v202\n"
      "v_mul_u32_u24 v176, 14, v208\n"
      "buffer_load_dwordx4 v249, s[4:7], s79 offen offset:1024 lds\n"
      "v_sub_u32 v184, v201, v176\n"
      "v_cmp_gt_u32 vcc, 12, v184\n"
      "s_nop 1\n"
      "v_cndmask_b32 v253, v215, v220, vcc\n"
      "v_lshl_add_u32 v254, v184, 4, v253\n"
      "v_mul_u32_u24 v252, 0xd80, v208\n"
      "v_add_u32 v255, v254, v252\n"
      "v_add_u32 v168, 0xfffff800, v255\n"
      "buffer_load_dwordx4 v246, s[4:7], s79 offen offset:2048 lds\n"
      "buffer_load_dwordx4 v244, s[4:7], s79 offen offset:3072 lds\n"
      "s_add_u32 s79, s79, 128\n"
      "s_cmp_eq_u32 s79, 1280\n"
      "s_cselect_b32 s79, 0, s79\n"
      "v_mov_b32 v4, 0x3f803f80\n"
      "v_mov_b32 v5, 0x3f803f80\n"
      "v_mov_b32 v6, 0x3f803f80\n"
      "v_mov_b32 v7, 0x3f803f80\n"
      "s_mul_i32 s87, s101, 3\n"
      "v_add_u32 v163, s87, v1\n"
      "v_mul_u32_u24 v164, 0x1830, v163\n"
      "v_add_u32 v231, 0x800, v164\n"
      "s_mov_b64 exec, 7\n"
      "ds_write_b128 v231, v[4:7]\n"
      "s_mov_b64 exec, -1\n"
      "s_lshl_b32 s35, s101, 9\n"
      "s_add_u32 s56, s35, 0x27000\n"
      "v_lshl_add_u32 v231, v1, 3, s56\n"
      "v_mov_b32 v4, 0\n"
      "v_mov_b32 v5, 0\n"
      "ds_write_b64 v231, v[4:5]\n"
      "s_mov_b32 s34, 0x3f803f80\n"
      "v_mov_b32 v116, 0\n"
      "v_mov_b32 v120, 0\n"
      "v_mov_b32 v117, 0\n"
      "v_mov_b32 v121, 0\n"
      "v_mov_b32 v118, 0\n"
      "v_mov_b32 v122, 0\n"
      "v_mov_b32 v119, 0\n"
      "v_mov_b32 v123, 0\n"
      "v_accvgpr_write_b32 a30, 0\n"
      "v_accvgpr_write_b32 a31, 0\n"
      "v_accvgpr_write_b32 a42, 0\n"
      "v_accvgpr_write_b32 a43, 0\n"
      "s_waitcnt vmcnt(13)\n"
      "s_waitcnt lgkmcnt(0)\n"
      "s_barrier\n"
      "ds_read_b128 v[100:103], v222 offset:0\n"
      "ds_read_b64_tr_b16 v[68:69], v214 offset:0\n"
      "ds_read_b64_tr_b16 v[70:71], v214 offset:512\n"
      "ds_read_b64_tr_b16 v[72:73], v214 offset:2064\n"
      "ds_read_b64_tr_b16 v[74:75], v214 offset:2576\n"
      "ds_read_b64_tr_b16 v[76:77], v214 offset:4128\n"
      "ds_read_b64_tr_b16 v[78:79], v214 offset:4640\n"
      "ds_read_b64_tr_b16 v[80:81], v213 offset:0\n"
      "ds_read_b64_tr_b16 v[82:83], v212 offset:0\n"
      "s_waitcnt lgkmcnt(6)\n"
      "v_mfma_f32_32x32x16_bf16 v[4:19], v[68:71], v[100:103], 0\n"
      "ds_read_b128 v[104:107], v211 offset:0\n"
      "ds_read_b64_tr_b16 v[84:85], v206 offset:0\n"
      "ds_read_b64_tr_b16 v[86:87], v206 offset:512\n"
      "s_mul_i32 s74, s79, 1920\n"
      "s_add_u32 m0, s90, 49536\n"
      "s_nop 0\n"
      "buffer_load_dwordx4 v237, s[8:11], s74 offen offset:0 lds\n"
      "s_waitcnt lgkmcnt(7)\n"
      "v_mfma_f32_32x32x16_bf16 v[20:35], v[72:75], v[100:103], 0\n"
      "ds_read_b64_tr_b16 v[88:89], v206 offset:2064\n"
      "ds_read_b64_tr_b16 v[90:91], v206 offset:2576\n"
      "ds_read_b64_tr_b16 v[92:93], v206 offset:4128\n"
      "s_waitcnt lgkmcnt(8)\n"
      "v_mfma_f32_32x32x16_bf16 v[36:51], v[76:79], v[100:103], 0\n"
      "ds_read_b64_tr_b16 v[94:95], v206 offset:4640\n"
      "ds_read_b64_tr_b16 v[96:97], v205 offset:0\n"
      "ds_read_b64_tr_b16 v[98:99], v204 offset:0\n"
      "buffer_load_dwordx4 v236, s[8:11], s74 offen offset:1024 lds\n"
      "s_waitcnt lgkmcnt(9)\n"
      "v_mfma_f32_32x32x16_bf16 v[52:67], v[80:83], v[100:103], 0\n"
      "s_waitcnt lgkmcnt(6)\n"
      "v_mfma_f32_32x32x16_bf16 v[4:19], v[84:87], v[104:107], v[4:19]\n"
      "ds_read_b128 v[108:111], v203 offset:0\n"
      "ds_read_b64_tr_b16 v[68:69], v198 offset:0\n"
      "ds_read_b64_tr_b16 v[70:71], v198 offset:512\n"
      "buffer_load_dwordx4 v235, s[8:11], s74 offen offset:2064 lds\n"
      "s_waitcnt lgkmcnt(7)\n"
      "v_mfma_f32_32x32x16_bf16 v[20:35], v[88:91], v[104:107], v[20:35]\n"
      "ds_read_b64_tr_b16 v[72:73], v198 offset:2064\n"
      "ds_read_b64_tr_b16 v[74:75], v198 offset:2576\n"
      "ds_read_b64_tr_b16 v[76:77], v198 offset:4128\n"
      "s_waitcnt lgkmcnt(8)\n"
      "v_mfma_f32_32x32x16_bf16 v[36:51], v[92:95], v[104:107], v[36:51]\n"
      "ds_read_b64_tr_b16 v[78:79], v198 offset:4640\n"
      "ds_read_b64_tr_b16 v[80:81], v197 offset:0\n"
      "ds_read_b64_tr_b16 v[82:83], v196 offset:0\n"
      "buffer_load_dwordx4 v234, s[8:11], s74 offen offset:3088 lds\n"
      "s_add_u32 m0, s90, 53664\n"
      "s_waitcnt lgkmcnt(9)\n"
      "v_mfma_f32_32x32x16_bf16 v[52:67], v[96:99], v[104:107], v[52:67]\n"
      "s_waitcnt lgkmcnt(6)\n"
      "v_mfma_f32_32x32x16_bf16 v[4:19], v[68:71], v[108:111], v[4:19]\n"
      "ds_read_b128 v[112:115], v195 offset:0\n"
      "ds_read_b64_tr_b16 v[84:85], v190 offset:0\n"
      "ds_read_b64_tr_b16 v[86:87], v190 offset:512\n"
      "buffer_load_dwordx4 v233, s[8:11], s74 offen offset:0 lds\n"
      "s_waitcnt lgkmcnt(7)\n"
      "v_mfma_f32_32x32x16_bf16 v[20:35], v[72:75], v[108:111], v[20:35]\n"
      "ds_read_b64_tr_b16 v[88:89], v190 offset:2064\n"
      "ds_read_b64_tr_b16 v[90:91], v190 offset:2576\n"
      "ds_read_b64_tr_b16 v[92:93], v190 offset:4128\n"
      "s_waitcnt lgkmcnt(8)\n"
      "v_mfma_f32_32x32x16_bf16 v[36:51], v[76:79], v[108:111], v[36:51]\n"
      "ds_read_b64_tr_b16 v[94:95], v190 offset:4640\n"
      "ds_read_b64_tr_b16 v[96:97], v189 offset:0\n"
      "ds_read_b64_tr_b16 v[98:99], v188 offset:0\n"
      "buffer_load_dwordx4 v232, s[8:11], s74 offen offset:1024 lds\n"
      "s_add_u32 m0, s98, 32768\n"
      "s_waitcnt lgkmcnt(9)\n"
      "v_mfma_f32_32x32x16_bf16 v[52:67], v[80:83], v[108:111], v[52:67]\n"
      "s_waitcnt lgkmcnt(0)\n"
      "v_dot2_f32_bf16 v116, v84, s34, v116\n"
      "v_dot2_f32_bf16 v120, v100, v100, v120\n"
      "v_dot2_f32_bf16 v117, v88, s34, v117\n"
      "v_dot2_f32_bf16 v121, v104, v104, v121\n"
      "v_dot2_f32_bf16 v118, v92, s34, v118\n"
      "v_dot2_f32_bf16 v122, v108, v108, v122\n"
      "v_dot2_f32_bf16 v119, v96, s34, v119\n"
      "v_dot2_f32_bf16 v123, v112, v112, v123\n"
      "buffer_load_dwordx4 v2, s[4:7], s79 offen offset:0 lds\n"
      "v_dot2_f32_bf16 v116, v85, s34, v116\n"
      "v_dot2_f32_bf16 v120, v101, v101, v120\n"
      "v_dot2_f32_bf16 v117, v89, s34, v117\n"
      "v_dot2_f32_bf16 v121, v105, v105, v121\n"
      "v_dot2_f32_bf16 v118, v93, s34, v118\n"
      "v_dot2_f32_bf16 v122, v109, v109, v122\n"
      "v_dot2_f32_bf16 v119, v97, s34, v119\n"
      "v_dot2_f32_bf16 v123, v113, v113, v123\n"
      "v_dot2_f32_bf16 v116, v86, s34, v116\n"
      "v_dot2_f32_bf16 v120, v102, v102, v120\n"
      "v_dot2_f32_bf16 v117, v90, s34, v117\n"
      "v_dot2_f32_bf16 v121, v106, v106, v121\n"
      "v_dot2_f32_bf16 v118, v94, s34, v118\n"
      "v_dot2_f32_bf16 v122, v110, v110, v122\n"
      "v_dot2_f32_bf16 v119, v98, s34, v119\n"
      "v_dot2_f32_bf16 v123, v114, v114, v123\n"
      "buffer_load_dwordx4 v249, s[4:7], s79 offen offset:1024 lds\n"
      "v_dot2_f32_bf16 v116, v87, s34, v116\n"
      "v_dot2_f32_bf16 v120, v103, v103, v120\n"
      "v_dot2_f32_bf16 v117, v91, s34, v117\n"
      "v_dot2_f32_bf16 v121, v107, v107, v121\n"
      "v_dot2_f32_bf16 v118, v95, s34, v118\n"
      "v_dot2_f32_bf16 v122, v111, v111, v122\n"
      "v_dot2_f32_bf16 v119, v99, s34, v119\n"
      "v_dot2_f32_bf16 v123, v115, v115, v123\n"
      "buffer_load_dwordx4 v246, s[4:7], s79 offen offset:2048 lds\n"
      "buffer_load_dwordx4 v244, s[4:7], s79 offen offset:3072 lds\n"
      "s_add_u32 s79, s79, 128\n"
      "s_cmp_eq_u32 s79, 1280\n"
      "s_cselect_b32 s79, 0, s79\n"
      "s_waitcnt vmcnt(10)\n"
      "s_waitcnt lgkmcnt(0)\n"
      "s_barrier\n"
      "v_mfma_f32_32x32x16_bf16 v[4:19], v[84:87], v[112:115], v[4:19]\n"
      "ds_read_b128 v[100:103], v222 offset:16384\n"
      "ds_read_b64_tr_b16 v[68:69], v214 offset:24768\n"
      "ds_read_b64_tr_b16 v[70:71], v214 offset:25280\n"
      "s_mul_i32 s74, s79, 1920\n"
      "s_add_u32 m0, s90, 0\n"
      "s_nop 0\n"
      "buffer_load_dwordx4 v237, s[8:11], s74 offen offset:0 lds\n"
      "v_mfma_f32_32x32x16_bf16 v[20:35], v[88:91], v[112:115], v[20:35]\n"
      "ds_read_b64_tr_b16 v[72:73], v214 offset:26832\n"
      "ds_read_b64_tr_b16 v[74:75], v214 offset:27344\n"
      "ds_read_b64_tr_b16 v[76:77], v214 offset:28896\n"
      "v_mfma_f32_32x32x16_bf16 v[36:51], v[92:95], v[112:115], v[36:51]\n"
      "ds_read_b64_tr_b16 v[78:79], v214 offset:29408\n"
      "ds_read_b64_tr_b16 v[80:81], v213 offset:24768\n"
      "ds_read_b64_tr_b16 v[82:83], v212 offset:24768\n"
      "buffer_load_dwordx4 v236, s[8:11], s74 offen offset:1024 lds\n"
      "v_mfma_f32_32x32x16_bf16 v[52:67], v[96:99], v[112:115], v[52:67]\n"
      "ds_read_b64_tr_b16 a[20:21], v187 offset:0\n"
      "ds_read_b64_tr_b16 a[22:23], v187 offset:160\n"
      "ds_read_b64_tr_b16 a[24:25], v187 offset:320\n"
      "ds_read_b64_tr_b16 a[26:27], v187 offset:480\n"
      "ds_read_b64_tr_b16 a[28:29], v178\n"
      "ds_read_b64_tr_b16 a[32:33], v174 offset:0\n"
      "ds_read_b64_tr_b16 a[34:35], v174 offset:160\n"
      "ds_read_b64_tr_b16 a[36:37], v174 offset:320\n"
      "ds_read_b64_tr_b16 a[38:39], v174 offset:480\n"
      "ds_read_b64_tr_b16 a[40:41], v171\n"
      "v_add_co_u32 v166, vcc, 1, v166\n"
      "v_addc_co_u32 v167, vcc, 0, v167, vcc\n"
      "s_waitcnt lgkmcnt(15)\n"
      "v_mfma_f32_32x32x16_bf16 v[4:19], v[68:71], v[100:103], v[4:19]\n"
      "ds_read_b128 v[104:107], v211 offset:16384\n"
      "ds_read_b64_tr_b16 v[84:85], v206 offset:24768\n"
      "ds_read_b64_tr_b16 v[86:87], v206 offset:25280\n"
      "buffer_load_dwordx4 v235, s[8:11], s74 offen offset:2064 lds\n"
      "s_waitcnt lgkmcnt(15)\n"
      "v_mfma_f32_32x32x16_bf16 v[20:35], v[72:75], v[100:103], v[20:35]\n"
      "ds_read_b64_tr_b16 v[88:89], v206 offset:26832\n"
      "ds_read_b64_tr_b16 v[90:91], v206 offset:27344\n"
      "ds_read_b64_tr_b16 v[92:93], v206 offset:28896\n"
      "v_mfma_f32_32x32x16_bf16 v[36:51], v[76:79], v[100:103], v[36:51]\n"
      "ds_read_b64_tr_b16 v[94:95], v206 offset:29408\n"
      "ds_read_b64_tr_b16 v[96:97], v205 offset:24768\n"
      "ds_read_b64_tr_b16 v[98:99], v204 offset:24768\n"
      "buffer_load_dwordx4 v234, s[8:11], s74 offen offset:3088 lds\n"
      "s_add_u32 m0, s90, 4128\n"
      "s_waitcnt lgkmcnt(15)\n"
      "v_mfma_f32_32x32x16_bf16 v[52:67], v[80:83], v[100:103], v[52:67]\n"
      "s_waitcnt lgkmcnt(6)\n"
      "v_mfma_f32_32x32x16_bf16 v[4:19], v[84:87], v[104:107], v[4:19]\n"
      "ds_read_b128 v[108:111], v203 offset:16384\n"
      "ds_read_b64_tr_b16 v[68:69], v198 offset:24768\n"
      "ds_read_b64_tr_b16 v[70:71], v198 offset:25280\n"
      "buffer_load_dwordx4 v233, s[8:11], s74 offen offset:0 lds\n"
      "s_waitcnt lgkmcnt(7)\n"
      "v_mfma_f32_32x32x16_bf16 v[20:35], v[88:91], v[104:107], v[20:35]\n"
      "ds_read_b64_tr_b16 v[72:73], v198 offset:26832\n"
      "ds_read_b64_tr_b16 v[74:75], v198 offset:27344\n"
      "ds_read_b64_tr_b16 v[76:77], v198 offset:28896\n"
      "s_waitcnt lgkmcnt(8)\n"
      "v_mfma_f32_32x32x16_bf16 v[36:51], v[92:95], v[104:107], v[36:51]\n"
      "ds_read_b64_tr_b16 v[78:79], v198 offset:29408\n"
      "ds_read_b64_tr_b16 v[80:81], v197 offset:24768\n"
      "ds_read_b64_tr_b16 v[82:83], v196 offset:24768\n"
      "buffer_load_dwordx4 v232, s[8:11], s74 offen offset:1024 lds\n"
      "s_add_u32 m0, s98, 0\n"
      "s_waitcnt lgkmcnt(9)\n"
      "v_mfma_f32_32x32x16_bf16 v[52:67], v[96:99], v[104:107], v[52:67]\n"
      "s_waitcnt lgkmcnt(6)\n"
      "v_mfma_f32_32x32x16_bf16 v[4:19], v[68:71], v[108:111], v[4:19]\n"
      "ds_read_b128 v[112:115], v195 offset:16384\n"
      "ds_read_b64_tr_b16 v[84:85], v190 offset:24768\n"
      "ds_read_b64_tr_b16 v[86:87], v190 offset:25280\n"
      "buffer_load_dwordx4 v2, s[4:7], s79 offen offset:0 lds\n"
      "s_waitcnt lgkmcnt(7)\n"
      "v_mfma_f32_32x32x16_bf16 v[20:35], v[72:75], v[108:111], v[20:35]\n"
      "ds_read_b64_tr_b16 v[88:89], v190 offset:26832\n"
      "ds_read_b64_tr_b16 v[90:91], v190 offset:27344\n"
      "ds_read_b64_tr_b16 v[92:93], v190 offset:28896\n"
      "s_waitcnt lgkmcnt(8)\n"
      "v_mfma_f32_32x32x16_bf16 v[36:51], v[76:79], v[108:111], v[36:51]\n"
      "ds_read_b64_tr_b16 v[94:95], v190 offset:29408\n"
      "ds_read_b64_tr_b16 v[96:97], v189 offset:24768\n"
      "ds_read_b64_tr_b16 v[98:99], v188 offset:24768\n"
      "buffer_load_dwordx4 v249, s[4:7], s79 offen offset:1024 lds\n"
      "s_waitcnt lgkmcnt(9)\n"
      "v_mfma_f32_32x32x16_bf16 v[52:67], v[80:83], v[108:111], v[52:67]\n"
      "s_waitcnt lgkmcnt(0)\n"
      "v_dot2_f32_bf16 v116, v84, s34, v116\n"
      "v_dot2_f32_bf16 v120, v100, v100, v120\n"
      "v_dot2_f32_bf16 v117, v88, s34, v117\n"
      "v_dot2_f32_bf16 v121, v104, v104, v121\n"
      "v_dot2_f32_bf16 v118, v92, s34, v118\n"
      "v_dot2_f32_bf16 v122, v108, v108, v122\n"
      "v_dot2_f32_bf16 v119, v96, s34, v119\n"
      "v_dot2_f32_bf16 v123, v112, v112, v123\n"
      "buffer_load_dwordx4 v246, s[4:7], s79 offen offset:2048 lds\n"
      "v_dot2_f32_bf16 v116, v85, s34, v116\n"
      "v_dot2_f32_bf16 v120, v101, v101, v120\n"
      "v_dot2_f32_bf16 v117, v89, s34, v117\n"
      "v_dot2_f32_bf16 v121, v105, v105, v121\n"
      "v_dot2_f32_bf16 v118, v93, s34, v118\n"
      "v_dot2_f32_bf16 v122, v109, v109, v122\n"
      "v_dot2_f32_bf16 v119, v97, s34, v119\n"
      "v_dot2_f32_bf16 v123, v113, v113, v123\n"
      "v_dot2_f32_bf16 v116, v86, s34, v116\n"
      "v_dot2_f32_bf16 v120, v102, v102, v120\n"
      "v_dot2_f32_bf16 v117, v90, s34, v117\n"
      "v_dot2_f32_bf16 v121, v106, v106, v121\n"
      "v_dot2_f32_bf16 v118, v94, s34, v118\n"
      "v_dot2_f32_bf16 v122, v110, v110, v122\n"
      "v_dot2_f32_bf16 v119, v98, s34, v119\n"
      "v_dot2_f32_bf16 v123, v114, v114, v123\n"
      "buffer_load_dwordx4 v244, s[4:7], s79 offen offset:3072 lds\n"
      "s_add_u32 s79, s79, 128\n"
      "s_cmp_eq_u32 s79, 1280\n"
      "s_cselect_b32 s79, 0, s79\n"
      "v_dot2_f32_bf16 v116, v87, s34, v116\n"
      "v_dot2_f32_bf16 v120, v103, v103, v120\n"
      "v_dot2_f32_bf16 v117, v91, s34, v117\n"
      "v_dot2_f32_bf16 v121, v107, v107, v121\n"
      "v_dot2_f32_bf16 v118, v95, s34, v118\n"
      "v_dot2_f32_bf16 v122, v111, v111, v122\n"
      "v_dot2_f32_bf16 v119, v99, s34, v119\n"
      "v_dot2_f32_bf16 v123, v115, v115, v123\n"
      "s_waitcnt vmcnt(10)\n"
      "s_waitcnt lgkmcnt(0)\n"
      "s_barrier\n"
      "v_mfma_f32_32x32x16_bf16 v[4:19], v[84:87], v[112:115], v[4:19]\n"
      "ds_read_b128 v[100:103], v222 offset:32768\n"
      "ds_read_b64_tr_b16 v[68:69], v214 offset:49536\n"
      "ds_read_b64_tr_b16 v[70:71], v214 offset:50048\n"
      "s_mul_i32 s74, s79, 1920\n"
      "s_add_u32 m0, s90, 24768\n"
      "s_nop 0\n"
      "buffer_load_dwordx4 v237, s[8:11], s74 offen offset:0 lds\n"
      "v_mfma_f32_32x32x16_bf16 v[20:35], v[88:91], v[112:115], v[20:35]\n"
      "ds_read_b64_tr_b16 v[72:73], v214 offset:51600\n"
      "ds_read_b64_tr_b16 v[74:75], v214 offset:52112\n"
      "ds_read_b64_tr_b16 v[76:77], v214 offset:53664\n"
      "v_mfma_f32_32x32x16_bf16 v[36:51], v[92:95], v[112:115], v[36:51]\n"
      "ds_read_b64_tr_b16 v[78:79], v214 offset:54176\n"
      "ds_read_b64_tr_b16 v[80:81], v213 offset:49536\n"
      "ds_read_b64_tr_b16 v[82:83], v212 offset:49536\n"
      "buffer_load_dwordx4 v236, s[8:11], s74 offen offset:1024 lds\n"
      "v_mfma_f32_32x32x16_bf16 v[52:67], v[96:99], v[112:115], v[52:67]\n"
      "s_waitcnt lgkmcnt(6)\n"
      "v_mfma_f32_32x32x16_bf16 v[4:19], v[68:71], v[100:103], v[4:19]\n"
      "ds_read_b128 v[104:107], v211 offset:32768\n"
      "ds_read_b64_tr_b16 v[84:85], v206 offset:49536\n"
      "ds_read_b64_tr_b16 v[86:87], v206 offset:50048\n"
      "buffer_load_dwordx4 v235, s[8:11], s74 offen offset:2064 lds\n"
      "s_waitcnt lgkmcnt(7)\n"
      "v_mfma_f32_32x32x16_bf16 v[20:35], v[72:75], v[100:103], v[20:35]\n"
      "ds_read_b64_tr_b16 v[88:89], v206 offset:51600\n"
      "ds_read_b64_tr_b16 v[90:91], v206 offset:52112\n"
      "ds_read_b64_tr_b16 v[92:93], v206 offset:53664\n"
      "s_waitcnt lgkmcnt(8)\n"
      "v_mfma_f32_32x32x16_bf16 v[36:51], v[76:79], v[100:103], v[36:51]\n"
      "ds_read_b64_tr_b16 v[94:95], v206 offset:54176\n"
      "ds_read_b64_tr_b16 v[96:97], v205 offset:49536\n"
      "ds_read_b64_tr_b16 v[98:99], v204 offset:49536\n"
      "buffer_load_dwordx4 v234, s[8:11], s74 offen offset:3088 lds\n"
      "s_add_u32 m0, s90, 28896\n"
      "s_waitcnt lgkmcnt(9)\n"
      "v_mfma_f32_32x32x16_bf16 v[52:67], v[80:83], v[100:103], v[52:67]\n"
      "s_waitcnt lgkmcnt(6)\n"
      "v_mfma_f32_32x32x16_bf16 v[4:19], v[84:87], v[104:107], v[4:19]\n"
      "ds_read_b128 v[108:111], v203 offset:32768\n"
      "ds_read_b64_tr_b16 v[68:69], v198 offset:49536\n"
      "ds_read_b64_tr_b16 v[70:71], v198 offset:50048\n"
      "buffer_load_dwordx4 v233, s[8:11], s74 offen offset:0 lds\n"
      "s_waitcnt lgkmcnt(7)\n"
      "v_mfma_f32_32x32x16_bf16 v[20:35], v[88:91], v[104:107], v[20:35]\n"
      "ds_read_b64_tr_b16 v[72:73], v198 offset:51600\n"
      "ds_read_b64_tr_b16 v[74:75], v198 offset:52112\n"
      "ds_read_b64_tr_b16 v[76:77], v198 offset:53664\n"
      "s_waitcnt lgkmcnt(8)\n"
      "v_mfma_f32_32x32x16_bf16 v[36:51], v[92:95], v[104:107], v[36:51]\n"
      "ds_read_b64_tr_b16 v[78:79], v198 offset:54176\n"
      "ds_read_b64_tr_b16 v[80:81], v197 offset:49536\n"
      "ds_read_b64_tr_b16 v[82:83], v196 offset:49536\n"
      "buffer_load_dwordx4 v232, s[8:11], s74 offen offset:1024 lds\n"
      "s_add_u32 m0, s98, 16384\n"
      "s_waitcnt lgkmcnt(9)\n"
      "v_mfma_f32_32x32x16_bf16 v[52:67], v[96:99], v[104:107], v[52:67]\n"
      "s_waitcnt lgkmcnt(6)\n"
      "v_mfma_f32_32x32x16_bf16 v[4:19], v[68:71], v[108:111], v[4:19]\n"
      "ds_read_b128 v[112:115], v195 offset:32768\n"
      "ds_read_b64_tr_b16 v[84:85], v190 offset:49536\n"
      "ds_read_b64_tr_b16 v[86:87], v190 offset:50048\n"
      "buffer_load_dwordx4 v2, s[4:7], s79 offen offset:0 lds\n"
      "s_waitcnt lgkmcnt(7)\n"
      "v_mfma_f32_32x32x16_bf16 v[20:35], v[72:75], v[108:111], v[20:35]\n"
      "ds_read_b64_tr_b16 v[88:89], v190 offset:51600\n"
      "ds_read_b64_tr_b16 v[90:91], v190 offset:52112\n"
      "ds_read_b64_tr_b16 v[92:93], v190 offset:53664\n"
      "s_waitcnt lgkmcnt(8)\n"
      "v_mfma_f32_32x32x16_bf16 v[36:51], v[76:79], v[108:111], v[36:51]\n"
      "ds_read_b64_tr_b16 v[94:95], v190 offset:54176\n"
      "ds_read_b64_tr_b16 v[96:97], v189 offset:49536\n"
      "ds_read_b64_tr_b16 v[98:99], v188 offset:49536\n"
      "buffer_load_dwordx4 v249, s[4:7], s79 offen offset:1024 lds\n"
      "s_waitcnt lgkmcnt(9)\n"
      "v_mfma_f32_32x32x16_bf16 v[52:67], v[80:83], v[108:111], v[52:67]\n"
      "v_bfe_u32 v220, v1, 2, 1\n"
      "v_lshrrev_b32 v202, 5, v1\n"
      "s_lshl_b32 s80, s101, 5\n"
      "v_and_b32 v184, 31, v1\n"
      "v_add_u32 v187, s80, v184\n"
      "v_mul_u32_u24 v201, 0x120, v187\n"
      "v_lshl_add_u32 v208, v202, 7, v201\n"
      "v_add_u32 v215, 0x19000, v208\n"
      "v_lshl_add_u32 v231, v220, 4, v215\n"
      "v_lshlrev_b32 v176, 4, v220\n"
      "v_sub_u32 v178, v215, v176\n"
      "s_lshl2_add_u32 s87, s101, 0x26000\n"
      "v_lshrrev_b32 v208, 5, v1\n"
      "v_and_b32 v201, 31, v1\n"
      "v_lshlrev_b32 v202, 4, v201\n"
      "v_lshl_add_u32 v215, v208, 9, v202\n"
      "v_add_u32 v220, s87, v215\n"
      "v_add_u32 v187, 0x26000, v202\n"
      "s_waitcnt lgkmcnt(0)\n"
      "v_dot2_f32_bf16 v116, v84, s34, v116\n"
      "v_dot2_f32_bf16 v120, v100, v100, v120\n"
      "v_dot2_f32_bf16 v117, v88, s34, v117\n"
      "v_dot2_f32_bf16 v121, v104, v104, v121\n"
      "v_dot2_f32_bf16 v118, v92, s34, v118\n"
      "v_dot2_f32_bf16 v122, v108, v108, v122\n"
      "v_dot2_f32_bf16 v119, v96, s34, v119\n"
      "v_dot2_f32_bf16 v123, v112, v112, v123\n"
      "buffer_load_dwordx4 v246, s[4:7], s79 offen offset:2048 lds\n"
      "v_dot2_f32_bf16 v116, v85, s34, v116\n"
      "v_dot2_f32_bf16 v120, v101, v101, v120\n"
      "v_dot2_f32_bf16 v117, v89, s34, v117\n"
      "v_dot2_f32_bf16 v121, v105, v105, v121\n"
      "v_dot2_f32_bf16 v118, v93, s34, v118\n"
      "v_dot2_f32_bf16 v122, v109, v109, v122\n"
      "v_dot2_f32_bf16 v119, v97, s34, v119\n"
      "v_dot2_f32_bf16 v123, v113, v113, v123\n"
      "v_dot2_f32_bf16 v116, v86, s34, v116\n"
      "v_dot2_f32_bf16 v120, v102, v102, v120\n"
      "v_dot2_f32_bf16 v117, v90, s34, v117\n"
      "v_dot2_f32_bf16 v121, v106, v106, v121\n"
      "v_dot2_f32_bf16 v118, v94, s34, v118\n"
      "v_dot2_f32_bf16 v122, v110, v110, v122\n"
      "v_dot2_f32_bf16 v119, v98, s34, v119\n"
      "v_dot2_f32_bf16 v123, v114, v114, v123\n"
      "buffer_load_dwordx4 v244, s[4:7], s79 offen offset:3072 lds\n"
      "s_add_u32 s79, s79, 128\n"
      "s_cmp_eq_u32 s79, 1280\n"
      "s_cselect_b32 s79, 0, s79\n"
      "v_dot2_f32_bf16 v116, v87, s34, v116\n"
      "v_dot2_f32_bf16 v120, v103, v103, v120\n"
      "v_dot2_f32_bf16 v117, v91, s34, v117\n"
      "v_dot2_f32_bf16 v121, v107, v107, v121\n"
      "v_dot2_f32_bf16 v118, v95, s34, v118\n"
      "v_dot2_f32_bf16 v122, v111, v111, v122\n"
      "v_dot2_f32_bf16 v119, v99, s34, v119\n"
      "v_dot2_f32_bf16 v123, v115, v115, v123\n"
      "s_waitcnt vmcnt(10)\n"
      "s_waitcnt lgkmcnt(0)\n"
      "s_barrier\n"
      "v_mfma_f32_32x32x16_bf16 v[4:19], v[84:87], v[112:115], v[4:19]\n"
      "ds_read_b128 v[100:103], v222 offset:0\n"
      "ds_read_b64_tr_b16 v[68:69], v214 offset:0\n"
      "ds_read_b64_tr_b16 v[70:71], v214 offset:512\n"
      "s_mul_i32 s74, s79, 1920\n"
      "s_add_u32 m0, s90, 49536\n"
      "s_nop 0\n"
      "buffer_load_dwordx4 v237, s[8:11], s74 offen offset:0 lds\n"
      "v_mfma_f32_32x32x16_bf16 v[20:35], v[88:91], v[112:115], v[20:35]\n"
      "ds_read_b64_tr_b16 v[72:73], v214 offset:2064\n"
      "ds_read_b64_tr_b16 v[74:75], v214 offset:2576\n"
      "ds_read_b64_tr_b16 v[76:77], v214 offset:4128\n"
      "v_mfma_f32_32x32x16_bf16 v[36:51], v[92:95], v[112:115], v[36:51]\n"
      "ds_read_b64_tr_b16 v[78:79], v214 offset:4640\n"
      "ds_read_b64_tr_b16 v[80:81], v213 offset:0\n"
      "ds_read_b64_tr_b16 v[82:83], v212 offset:0\n"
      "buffer_load_dwordx4 v236, s[8:11], s74 offen offset:1024 lds\n"
      "v_mfma_f32_32x32x16_bf16 v[52:67], v[96:99], v[112:115], v[52:67]\n"
      "s_waitcnt lgkmcnt(6)\n"
      "v_mfma_f32_32x32x16_bf16 v[4:19], v[68:71], v[100:103], v[4:19]\n"
      "ds_read_b128 v[104:107], v211 offset:0\n"
      "ds_read_b64_tr_b16 v[84:85], v206 offset:0\n"
      "ds_read_b64_tr_b16 v[86:87], v206 offset:512\n"
      "buffer_load_dwordx4 v235, s[8:11], s74 offen offset:2064 lds\n"
      "s_waitcnt lgkmcnt(7)\n"
      "v_mfma_f32_32x32x16_bf16 v[20:35], v[72:75], v[100:103], v[20:35]\n"
      "ds_read_b64_tr_b16 v[88:89], v206 offset:2064\n"
      "ds_read_b64_tr_b16 v[90:91], v206 offset:2576\n"
      "ds_read_b64_tr_b16 v[92:93], v206 offset:4128\n"
      "s_waitcnt lgkmcnt(8)\n"
      "v_mfma_f32_32x32x16_bf16 v[36:51], v[76:79], v[100:103], v[36:51]\n"
      "ds_read_b64_tr_b16 v[94:95], v206 offset:4640\n"
      "ds_read_b64_tr_b16 v[96:97], v205 offset:0\n"
      "ds_read_b64_tr_b16 v[98:99], v204 offset:0\n"
      "buffer_load_dwordx4 v234, s[8:11], s74 offen offset:3088 lds\n"
      "s_add_u32 m0, s90, 53664\n"
      "s_waitcnt lgkmcnt(9)\n"
      "v_mfma_f32_32x32x16_bf16 v[52:67], v[80:83], v[100:103], v[52:67]\n"
      "s_waitcnt lgkmcnt(6)\n"
      "v_mfma_f32_32x32x16_bf16 v[4:19], v[84:87], v[104:107], v[4:19]\n"
      "ds_read_b128 v[108:111], v203 offset:0\n"
      "ds_read_b64_tr_b16 v[68:69], v198 offset:0\n"
      "ds_read_b64_tr_b16 v[70:71], v198 offset:512\n"
      "buffer_load_dwordx4 v233, s[8:11], s74 offen offset:0 lds\n"
      "s_waitcnt lgkmcnt(7)\n"
      "v_mfma_f32_32x32x16_bf16 v[20:35], v[88:91], v[104:107], v[20:35]\n"
      "ds_read_b64_tr_b16 v[72:73], v198 offset:2064\n"
      "ds_read_b64_tr_b16 v[74:75], v198 offset:2576\n"
      "ds_read_b64_tr_b16 v[76:77], v198 offset:4128\n"
      "s_waitcnt lgkmcnt(8)\n"
      "v_mfma_f32_32x32x16_bf16 v[36:51], v[92:95], v[104:107], v[36:51]\n"
      "ds_read_b64_tr_b16 v[78:79], v198 offset:4640\n"
      "ds_read_b64_tr_b16 v[80:81], v197 offset:0\n"
      "ds_read_b64_tr_b16 v[82:83], v196 offset:0\n"
      "buffer_load_dwordx4 v232, s[8:11], s74 offen offset:1024 lds\n"
      "s_add_u32 m0, s98, 32768\n"
      "s_waitcnt lgkmcnt(9)\n"
      "v_mfma_f32_32x32x16_bf16 v[52:67], v[96:99], v[104:107], v[52:67]\n"
      "s_waitcnt lgkmcnt(6)\n"
      "v_mfma_f32_32x32x16_bf16 v[4:19], v[68:71], v[108:111], v[4:19]\n"
      "ds_read_b128 v[112:115], v195 offset:0\n"
      "ds_read_b64_tr_b16 v[84:85], v190 offset:0\n"
      "ds_read_b64_tr_b16 v[86:87], v190 offset:512\n"
      "buffer_load_dwordx4 v2, s[4:7], s79 offen offset:0 lds\n"
      "s_waitcnt lgkmcnt(7)\n"
      "v_mfma_f32_32x32x16_bf16 v[20:35], v[72:75], v[108:111], v[20:35]\n"
      "ds_read_b64_tr_b16 v[88:89], v190 offset:2064\n"
      "ds_read_b64_tr_b16 v[90:91], v190 offset:2576\n"
      "ds_read_b64_tr_b16 v[92:93], v190 offset:4128\n"
      "s_waitcnt lgkmcnt(8)\n"
      "v_mfma_f32_32x32x16_bf16 v[36:51], v[76:79], v[108:111], v[36:51]\n"
      "ds_read_b64_tr_b16 v[94:95], v190 offset:4640\n"
      "ds_read_b64_tr_b16 v[96:97], v189 offset:0\n"
      "ds_read_b64_tr_b16 v[98:99], v188 offset:0\n"
      "buffer_load_dwordx4 v249, s[4:7], s79 offen offset:1024 lds\n"
      "s_waitcnt lgkmcnt(9)\n"
      "v_mfma_f32_32x32x16_bf16 v[52:67], v[80:83], v[108:111], v[52:67]\n"
      "v_bfe_u32 v202, v1, 4, 1\n"
      "v_bfe_u32 v201, v1, 1, 1\n"
      "v_xor_b32 v208, v202, v201\n"
      "s_lshr_b32 s75, s101, 1\n"
      "s_lshl_b32 s80, s75, 7\n"
      "s_add_u32 s87, s80, 0x19000\n"
      "v_and_b32 v174, 1, v1\n"
      "v_bfe_u32 v164, v1, 2, 2\n"
      "v_lshrrev_b32 v253, 4, v1\n"
      "v_lshrrev_b32 v254, 1, v253\n"
      "v_lshlrev_b32 v252, 2, v202\n"
      "v_lshl_add_u32 v255, v254, 4, v252\n"
      "v_add_u32 v165, v164, v255\n"
      "v_mul_u32_u24 v171, 0x120, v165\n"
      "v_lshl_add_u32 v176, v174, 3, v171\n"
      "v_add_u32 v184, s87, v176\n"
      "v_lshl_add_u32 v215, v208, 4, v184\n"
      "s_and_b32 s35, s101, 1\n"
      "s_lshl1_add_u32 s70, s35, 5\n"
      "s_sub_u32 s69, 1, s35\n"
      "v_mul_u32_u24 v240, s69, v201\n"
      "v_add_u32 v241, s70, v240\n"
      "v_xor_b32 v242, v202, v241\n"
      "v_lshl_add_u32 v243, v242, 4, v184\n"
      "v_mul_u32_u24 v225, 0x150, v253\n"
      "s_lshl_b32 s68, s35, 4\n"
      "v_and_b32 v218, 15, v1\n"
      "v_add_u32 v219, s68, v218\n"
      "s_mul_i32 s64, s75, 0x690\n"
      "s_add_u32 s65, s64, 0x22000\n"
      "v_lshl_add_u32 v224, v219, 4, s65\n"
      "v_add_u32 v226, v225, v224\n"
      "v_and_b32 v216, 31, v1\n"
      "s_lshl_b32 s62, s101, 9\n"
      "s_add_u32 s63, s62, 0x26000\n"
      "v_lshl_add_u32 v217, v216, 4, s63\n"
      "v_cmp_ge_u32 vcc, 20, v219\n"
      "s_nop 1\n"
      "v_cndmask_b32 v227, v217, v226, vcc\n"
      "v_lshl_add_u32 v181, v253, 3, v224\n"
      "v_add_u32 v182, 0x540, v181\n"
      "v_lshl_add_u32 v180, v254, 5, v219\n"
      "v_cmp_ge_u32 vcc, 20, v180\n"
      "s_nop 1\n"
      "v_cndmask_b32 v183, v217, v182, vcc\n"
      "s_lshl_b32 s60, s101, 10\n"
      "s_add_u32 s61, s60, 0x22000\n"
      "v_add_u32 v163, s61, v3\n"
      "s_lshl4_add_u32 s56, s86, s88\n"
      "s_mul_i32 s57, s56, 0xe00\n"
      "s_add_u32 s58, s60, s57\n"
      "s_add_u32 s59, s58, 0x71000\n"
      "v_add_u32 v161, s59, v3\n"
      "v_mov_b32 v160, 0x40000000\n"
      "s_lshl_b32 s55, s101, 6\n"
      "v_add_u32 v159, s55, v1\n"
      "v_cmp_gt_u32 vcc, 0xe0, v159\n"
      "s_nop 1\n"
      "v_cndmask_b32 v162, v160, v161, vcc\n"
      "s_lshl_b32 s52, s86, 11\n"
      "s_lshl3_add_u32 s53, s88, s52\n"
      "s_add_u32 s54, s53, 0x1000\n"
      "v_lshl_add_u32 v157, v1, 7, s54\n"
      "v_cmp_gt_u32 vcc, 16, v159\n"
      "s_nop 1\n"
      "v_cndmask_b32 v158, v160, v157, vcc\n"
      "v_accvgpr_write_b32 a255, v215\n"
      "v_accvgpr_write_b32 a254, v243\n"
      "v_accvgpr_write_b32 a253, v227\n"
      "v_accvgpr_write_b32 a252, v183\n"
      "v_accvgpr_write_b32 a251, v163\n"
      "v_accvgpr_write_b32 a250, v162\n"
      "v_accvgpr_write_b32 a249, v158\n"
      "s_waitcnt lgkmcnt(0)\n"
      "v_dot2_f32_bf16 v116, v84, s34, v116\n"
      "v_dot2_f32_bf16 v120, v100, v100, v120\n"
      "v_dot2_f32_bf16 v117, v88, s34, v117\n"
      "v_dot2_f32_bf16 v121, v104, v104, v121\n"
      "v_dot2_f32_bf16 v118, v92, s34, v118\n"
      "v_dot2_f32_bf16 v122, v108, v108, v122\n"
      "v_dot2_f32_bf16 v119, v96, s34, v119\n"
      "v_dot2_f32_bf16 v123, v112, v112, v123\n"
      "buffer_load_dwordx4 v246, s[4:7], s79 offen offset:2048 lds\n"
      "v_dot2_f32_bf16 v116, v85, s34, v116\n"
      "v_dot2_f32_bf16 v120, v101, v101, v120\n"
      "v_dot2_f32_bf16 v117, v89, s34, v117\n"
      "v_dot2_f32_bf16 v121, v105, v105, v121\n"
      "v_dot2_f32_bf16 v118, v93, s34, v118\n"
      "v_dot2_f32_bf16 v122, v109, v109, v122\n"
      "v_dot2_f32_bf16 v119, v97, s34, v119\n"
      "v_dot2_f32_bf16 v123, v113, v113, v123\n"
      "v_dot2_f32_bf16 v116, v86, s34, v116\n"
      "v_dot2_f32_bf16 v120, v102, v102, v120\n"
      "v_dot2_f32_bf16 v117, v90, s34, v117\n"
      "v_dot2_f32_bf16 v121, v106, v106, v121\n"
      "v_dot2_f32_bf16 v118, v94, s34, v118\n"
      "v_dot2_f32_bf16 v122, v110, v110, v122\n"
      "v_dot2_f32_bf16 v119, v98, s34, v119\n"
      "v_dot2_f32_bf16 v123, v114, v114, v123\n"
      "buffer_load_dwordx4 v244, s[4:7], s79 offen offset:3072 lds\n"
      "s_add_u32 s79, s79, 128\n"
      "s_cmp_eq_u32 s79, 1280\n"
      "s_cselect_b32 s79, 0, s79\n"
      "v_dot2_f32_bf16 v116, v87, s34, v116\n"
      "v_dot2_f32_bf16 v120, v103, v103, v120\n"
      "v_dot2_f32_bf16 v117, v91, s34, v117\n"
      "v_dot2_f32_bf16 v121, v107, v107, v121\n"
      "v_dot2_f32_bf16 v118, v95, s34, v118\n"
      "v_dot2_f32_bf16 v122, v111, v111, v122\n"
      "v_dot2_f32_bf16 v119, v99, s34, v119\n"
      "v_dot2_f32_bf16 v123, v115, v115, v123\n"
      "s_waitcnt vmcnt(10)\n"
      "s_waitcnt lgkmcnt(0)\n"
      "s_barrier\n"
      "v_mfma_f32_32x32x16_bf16 v[4:19], v[84:87], v[112:115], v[4:19]\n"
      "ds_read_b128 v[100:103], v222 offset:16384\n"
      "ds_read_b64_tr_b16 v[68:69], v214 offset:24768\n"
      "ds_read_b64_tr_b16 v[70:71], v214 offset:25280\n"
      "s_mul_i32 s74, s79, 1920\n"
      "s_add_u32 m0, s90, 0\n"
      "s_nop 0\n"
      "buffer_load_dwordx4 v237, s[8:11], s74 offen offset:0 lds\n"
      "v_mfma_f32_32x32x16_bf16 v[20:35], v[88:91], v[112:115], v[20:35]\n"
      "ds_read_b64_tr_b16 v[72:73], v214 offset:26832\n"
      "ds_read_b64_tr_b16 v[74:75], v214 offset:27344\n"
      "ds_read_b64_tr_b16 v[76:77], v214 offset:28896\n"
      "v_mfma_f32_32x32x16_bf16 v[36:51], v[92:95], v[112:115], v[36:51]\n"
      "ds_read_b64_tr_b16 v[78:79], v214 offset:29408\n"
      "ds_read_b64_tr_b16 v[80:81], v213 offset:24768\n"
      "ds_read_b64_tr_b16 v[82:83], v212 offset:24768\n"
      "buffer_load_dwordx4 v236, s[8:11], s74 offen offset:1024 lds\n"
      "v_mfma_f32_32x32x16_bf16 v[52:67], v[96:99], v[112:115], v[52:67]\n"
      "s_waitcnt lgkmcnt(6)\n"
      "v_mfma_f32_32x32x16_bf16 v[4:19], v[68:71], v[100:103], v[4:19]\n"
      "ds_read_b128 v[104:107], v211 offset:16384\n"
      "ds_read_b64_tr_b16 v[84:85], v206 offset:24768\n"
      "ds_read_b64_tr_b16 v[86:87], v206 offset:25280\n"
      "buffer_load_dwordx4 v235, s[8:11], s74 offen offset:2064 lds\n"
      "s_waitcnt lgkmcnt(7)\n"
      "v_mfma_f32_32x32x16_bf16 v[20:35], v[72:75], v[100:103], v[20:35]\n"
      "ds_read_b64_tr_b16 v[88:89], v206 offset:26832\n"
      "ds_read_b64_tr_b16 v[90:91], v206 offset:27344\n"
      "ds_read_b64_tr_b16 v[92:93], v206 offset:28896\n"
      "s_waitcnt lgkmcnt(8)\n"
      "v_mfma_f32_32x32x16_bf16 v[36:51], v[76:79], v[100:103], v[36:51]\n"
      "ds_read_b64_tr_b16 v[94:95], v206 offset:29408\n"
      "ds_read_b64_tr_b16 v[96:97], v205 offset:24768\n"
      "ds_read_b64_tr_b16 v[98:99], v204 offset:24768\n"
      "buffer_load_dwordx4 v234, s[8:11], s74 offen offset:3088 lds\n"
      "s_add_u32 m0, s90, 4128\n"
      "s_waitcnt lgkmcnt(9)\n"
      "v_mfma_f32_32x32x16_bf16 v[52:67], v[80:83], v[100:103], v[52:67]\n"
      "s_waitcnt lgkmcnt(6)\n"
      "v_mfma_f32_32x32x16_bf16 v[4:19], v[84:87], v[104:107], v[4:19]\n"
      "ds_read_b128 v[108:111], v203 offset:16384\n"
      "ds_read_b64_tr_b16 v[68:69], v198 offset:24768\n"
      "ds_read_b64_tr_b16 v[70:71], v198 offset:25280\n"
      "buffer_load_dwordx4 v233, s[8:11], s74 offen offset:0 lds\n"
      "s_waitcnt lgkmcnt(7)\n"
      "v_mfma_f32_32x32x16_bf16 v[20:35], v[88:91], v[104:107], v[20:35]\n"
      "ds_read_b64_tr_b16 v[72:73], v198 offset:26832\n"
      "ds_read_b64_tr_b16 v[74:75], v198 offset:27344\n"
      "ds_read_b64_tr_b16 v[76:77], v198 offset:28896\n"
      "s_waitcnt lgkmcnt(8)\n"
      "v_mfma_f32_32x32x16_bf16 v[36:51], v[92:95], v[104:107], v[36:51]\n"
      "ds_read_b64_tr_b16 v[78:79], v198 offset:29408\n"
      "ds_read_b64_tr_b16 v[80:81], v197 offset:24768\n"
      "ds_read_b64_tr_b16 v[82:83], v196 offset:24768\n"
      "buffer_load_dwordx4 v232, s[8:11], s74 offen offset:1024 lds\n"
      "s_add_u32 m0, s98, 0\n"
      "s_waitcnt lgkmcnt(9)\n"
      "v_mfma_f32_32x32x16_bf16 v[52:67], v[96:99], v[104:107], v[52:67]\n"
      "s_waitcnt lgkmcnt(6)\n"
      "v_mfma_f32_32x32x16_bf16 v[4:19], v[68:71], v[108:111], v[4:19]\n"
      "ds_read_b128 v[112:115], v195 offset:16384\n"
      "ds_read_b64_tr_b16 v[84:85], v190 offset:24768\n"
      "ds_read_b64_tr_b16 v[86:87], v190 offset:25280\n"
      "buffer_load_dwordx4 v2, s[4:7], s79 offen offset:0 lds\n"
      "s_waitcnt lgkmcnt(7)\n"
      "v_mfma_f32_32x32x16_bf16 v[20:35], v[72:75], v[108:111], v[20:35]\n"
      "ds_read_b64_tr_b16 v[88:89], v190 offset:26832\n"
      "ds_read_b64_tr_b16 v[90:91], v190 offset:27344\n"
      "ds_read_b64_tr_b16 v[92:93], v190 offset:28896\n"
      "s_waitcnt lgkmcnt(8)\n"
      "v_mfma_f32_32x32x16_bf16 v[36:51], v[76:79], v[108:111], v[36:51]\n"
      "ds_read_b64_tr_b16 v[94:95], v190 offset:29408\n"
      "ds_read_b64_tr_b16 v[96:97], v189 offset:24768\n"
      "ds_read_b64_tr_b16 v[98:99], v188 offset:24768\n"
      "buffer_load_dwordx4 v249, s[4:7], s79 offen offset:1024 lds\n"
      "s_waitcnt lgkmcnt(9)\n"
      "v_mfma_f32_32x32x16_bf16 v[52:67], v[80:83], v[108:111], v[52:67]\n"
      "s_waitcnt lgkmcnt(0)\n"
      "v_dot2_f32_bf16 v116, v84, s34, v116\n"
      "v_dot2_f32_bf16 v120, v100, v100, v120\n"
      "v_dot2_f32_bf16 v117, v88, s34, v117\n"
      "v_dot2_f32_bf16 v121, v104, v104, v121\n"
      "v_dot2_f32_bf16 v118, v92, s34, v118\n"
      "v_dot2_f32_bf16 v122, v108, v108, v122\n"
      "v_dot2_f32_bf16 v119, v96, s34, v119\n"
      "v_dot2_f32_bf16 v123, v112, v112, v123\n"
      "buffer_load_dwordx4 v246, s[4:7], s79 offen offset:2048 lds\n"
      "v_dot2_f32_bf16 v116, v85, s34, v116\n"
      "v_dot2_f32_bf16 v120, v101, v101, v120\n"
      "v_dot2_f32_bf16 v117, v89, s34, v117\n"
      "v_dot2_f32_bf16 v121, v105, v105, v121\n"
      "v_dot2_f32_bf16 v118, v93, s34, v118\n"
      "v_dot2_f32_bf16 v122, v109, v109, v122\n"
      "v_dot2_f32_bf16 v119, v97, s34, v119\n"
      "v_dot2_f32_bf16 v123, v113, v113, v123\n"
      "v_dot2_f32_bf16 v116, v86, s34, v116\n"
      "v_dot2_f32_bf16 v120, v102, v102, v120\n"
      "v_dot2_f32_bf16 v117, v90, s34, v117\n"
      "v_dot2_f32_bf16 v121, v106, v106, v121\n"
      "v_dot2_f32_bf16 v118, v94, s34, v118\n"
      "v_dot2_f32_bf16 v122, v110, v110, v122\n"
      "v_dot2_f32_bf16 v119, v98, s34, v119\n"
      "v_dot2_f32_bf16 v123, v114, v114, v123\n"
      "buffer_load_dwordx4 v244, s[4:7], s79 offen offset:3072 lds\n"
      "s_add_u32 s79, s79, 128\n"
      "s_cmp_eq_u32 s79, 1280\n"
      "s_cselect_b32 s79, 0, s79\n"
      "v_dot2_f32_bf16 v116, v87, s34, v116\n"
      "v_dot2_f32_bf16 v120, v103, v103, v120\n"
      "v_dot2_f32_bf16 v117, v91, s34, v117\n"
      "v_dot2_f32_bf16 v121, v107, v107, v121\n"
      "v_dot2_f32_bf16 v118, v95, s34, v118\n"
      "v_dot2_f32_bf16 v122, v111, v111, v122\n"
      "v_dot2_f32_bf16 v119, v99, s34, v119\n"
      "v_dot2_f32_bf16 v123, v115, v115, v123\n"
      "s_waitcnt vmcnt(10)\n"
      "s_waitcnt lgkmcnt(0)\n"
      "s_barrier\n"
      "v_mfma_f32_32x32x16_bf16 v[4:19], v[84:87], v[112:115], v[4:19]\n"
      "ds_read_b128 v[100:103], v222 offset:32768\n"
      "ds_read_b64_tr_b16 v[68:69], v214 offset:49536\n"
      "ds_read_b64_tr_b16 v[70:71], v214 offset:50048\n"
      "s_mul_i32 s74, s79, 1920\n"
      "s_add_u32 m0, s90, 24768\n"
      "s_nop 0\n"
      "buffer_load_dwordx4 v237, s[8:11], s74 offen offset:0 lds\n"
      "v_mfma_f32_32x32x16_bf16 v[20:35], v[88:91], v[112:115], v[20:35]\n"
      "ds_read_b64_tr_b16 v[72:73], v214 offset:51600\n"
      "ds_read_b64_tr_b16 v[74:75], v214 offset:52112\n"
      "ds_read_b64_tr_b16 v[76:77], v214 offset:53664\n"
      "v_mfma_f32_32x32x16_bf16 v[36:51], v[92:95], v[112:115], v[36:51]\n"
      "ds_read_b64_tr_b16 v[78:79], v214 offset:54176\n"
      "ds_read_b64_tr_b16 v[80:81], v213 offset:49536\n"
      "ds_read_b64_tr_b16 v[82:83], v212 offset:49536\n"
      "buffer_load_dwordx4 v236, s[8:11], s74 offen offset:1024 lds\n"
      "v_mfma_f32_32x32x16_bf16 v[52:67], v[96:99], v[112:115], v[52:67]\n"
      "s_waitcnt lgkmcnt(6)\n"
      "v_mfma_f32_32x32x16_bf16 v[4:19], v[68:71], v[100:103], v[4:19]\n"
      "ds_read_b128 v[104:107], v211 offset:32768\n"
      "ds_read_b64_tr_b16 v[84:85], v206 offset:49536\n"
      "ds_read_b64_tr_b16 v[86:87], v206 offset:50048\n"
      "buffer_load_dwordx4 v235, s[8:11], s74 offen offset:2064 lds\n"
      "s_waitcnt lgkmcnt(7)\n"
      "v_mfma_f32_32x32x16_bf16 v[20:35], v[72:75], v[100:103], v[20:35]\n"
      "ds_read_b64_tr_b16 v[88:89], v206 offset:51600\n"
      "ds_read_b64_tr_b16 v[90:91], v206 offset:52112\n"
      "ds_read_b64_tr_b16 v[92:93], v206 offset:53664\n"
      "s_waitcnt lgkmcnt(8)\n"
      "v_mfma_f32_32x32x16_bf16 v[36:51], v[76:79], v[100:103], v[36:51]\n"
      "ds_read_b64_tr_b16 v[94:95], v206 offset:54176\n"
      "ds_read_b64_tr_b16 v[96:97], v205 offset:49536\n"
      "ds_read_b64_tr_b16 v[98:99], v204 offset:49536\n"
      "buffer_load_dwordx4 v234, s[8:11], s74 offen offset:3088 lds\n"
      "s_add_u32 m0, s90, 28896\n"
      "s_waitcnt lgkmcnt(9)\n"
      "v_mfma_f32_32x32x16_bf16 v[52:67], v[80:83], v[100:103], v[52:67]\n"
      "s_waitcnt lgkmcnt(6)\n"
      "v_mfma_f32_32x32x16_bf16 v[4:19], v[84:87], v[104:107], v[4:19]\n"
      "ds_read_b128 v[108:111], v203 offset:32768\n"
      "ds_read_b64_tr_b16 v[68:69], v198 offset:49536\n"
      "ds_read_b64_tr_b16 v[70:71], v198 offset:50048\n"
      "buffer_load_dwordx4 v233, s[8:11], s74 offen offset:0 lds\n"
      "s_waitcnt lgkmcnt(7)\n"
      "v_mfma_f32_32x32x16_bf16 v[20:35], v[88:91], v[104:107], v[20:35]\n"
      "ds_read_b64_tr_b16 v[72:73], v198 offset:51600\n"
      "ds_read_b64_tr_b16 v[74:75], v198 offset:52112\n"
      "ds_read_b64_tr_b16 v[76:77], v198 offset:53664\n"
      "s_waitcnt lgkmcnt(8)\n"
      "v_mfma_f32_32x32x16_bf16 v[36:51], v[92:95], v[104:107], v[36:51]\n"
      "ds_read_b64_tr_b16 v[78:79], v198 offset:54176\n"
      "ds_read_b64_tr_b16 v[80:81], v197 offset:49536\n"
      "ds_read_b64_tr_b16 v[82:83], v196 offset:49536\n"
      "buffer_load_dwordx4 v232, s[8:11], s74 offen offset:1024 lds\n"
      "s_add_u32 m0, s98, 16384\n"
      "s_waitcnt lgkmcnt(9)\n"
      "v_mfma_f32_32x32x16_bf16 v[52:67], v[96:99], v[104:107], v[52:67]\n"
      "s_waitcnt lgkmcnt(6)\n"
      "v_mfma_f32_32x32x16_bf16 v[4:19], v[68:71], v[108:111], v[4:19]\n"
      "ds_read_b128 v[112:115], v195 offset:32768\n"
      "ds_read_b64_tr_b16 v[84:85], v190 offset:49536\n"
      "ds_read_b64_tr_b16 v[86:87], v190 offset:50048\n"
      "buffer_load_dwordx4 v2, s[4:7], s79 offen offset:0 lds\n"
      "s_waitcnt lgkmcnt(7)\n"
      "v_mfma_f32_32x32x16_bf16 v[20:35], v[72:75], v[108:111], v[20:35]\n"
      "ds_read_b64_tr_b16 v[88:89], v190 offset:51600\n"
      "ds_read_b64_tr_b16 v[90:91], v190 offset:52112\n"
      "ds_read_b64_tr_b16 v[92:93], v190 offset:53664\n"
      "s_waitcnt lgkmcnt(8)\n"
      "v_mfma_f32_32x32x16_bf16 v[36:51], v[76:79], v[108:111], v[36:51]\n"
      "ds_read_b64_tr_b16 v[94:95], v190 offset:54176\n"
      "ds_read_b64_tr_b16 v[96:97], v189 offset:49536\n"
      "ds_read_b64_tr_b16 v[98:99], v188 offset:49536\n"
      "buffer_load_dwordx4 v249, s[4:7], s79 offen offset:1024 lds\n"
      "s_waitcnt lgkmcnt(9)\n"
      "v_mfma_f32_32x32x16_bf16 v[52:67], v[80:83], v[108:111], v[52:67]\n"
      "s_waitcnt lgkmcnt(0)\n"
      "v_dot2_f32_bf16 v116, v84, s34, v116\n"
      "v_dot2_f32_bf16 v120, v100, v100, v120\n"
      "v_dot2_f32_bf16 v117, v88, s34, v117\n"
      "v_dot2_f32_bf16 v121, v104, v104, v121\n"
      "v_dot2_f32_bf16 v118, v92, s34, v118\n"
      "v_dot2_f32_bf16 v122, v108, v108, v122\n"
      "v_dot2_f32_bf16 v119, v96, s34, v119\n"
      "v_dot2_f32_bf16 v123, v112, v112, v123\n"
      "buffer_load_dwordx4 v246, s[4:7], s79 offen offset:2048 lds\n"
      "v_dot2_f32_bf16 v116, v85, s34, v116\n"
      "v_dot2_f32_bf16 v120, v101, v101, v120\n"
      "v_dot2_f32_bf16 v117, v89, s34, v117\n"
      "v_dot2_f32_bf16 v121, v105, v105, v121\n"
      "v_dot2_f32_bf16 v118, v93, s34, v118\n"
      "v_dot2_f32_bf16 v122, v109, v109, v122\n"
      "v_dot2_f32_bf16 v119, v97, s34, v119\n"
      "v_dot2_f32_bf16 v123, v113, v113, v123\n"
      "v_dot2_f32_bf16 v116, v86, s34, v116\n"
      "v_dot2_f32_bf16 v120, v102, v102, v120\n"
      "v_dot2_f32_bf16 v117, v90, s34, v117\n"
      "v_dot2_f32_bf16 v121, v106, v106, v121\n"
      "v_dot2_f32_bf16 v118, v94, s34, v118\n"
      "v_dot2_f32_bf16 v122, v110, v110, v122\n"
      "v_dot2_f32_bf16 v119, v98, s34, v119\n"
      "v_dot2_f32_bf16 v123, v114, v114, v123\n"
      "buffer_load_dwordx4 v244, s[4:7], s79 offen offset:3072 lds\n"
      "s_add_u32 s79, s79, 128\n"
      "s_cmp_eq_u32 s79, 1280\n"
      "s_cselect_b32 s79, 0, s79\n"
      "v_dot2_f32_bf16 v116, v87, s34, v116\n"
      "v_dot2_f32_bf16 v120, v103, v103, v120\n"
      "v_dot2_f32_bf16 v117, v91, s34, v117\n"
      "v_dot2_f32_bf16 v121, v107, v107, v121\n"
      "v_dot2_f32_bf16 v118, v95, s34, v118\n"
      "v_dot2_f32_bf16 v122, v111, v111, v122\n"
      "v_dot2_f32_bf16 v119, v99, s34, v119\n"
      "v_dot2_f32_bf16 v123, v115, v115, v123\n"
      "s_waitcnt vmcnt(10)\n"
      "s_waitcnt lgkmcnt(0)\n"
      "s_barrier\n"
      "v_mfma_f32_32x32x16_bf16 v[4:19], v[84:87], v[112:115], v[4:19]\n"
      "ds_read_b128 v[100:103], v222 offset:0\n"
      "ds_read_b64_tr_b16 v[68:69], v214 offset:0\n"
      "ds_read_b64_tr_b16 v[70:71], v214 offset:512\n"
      "s_mul_i32 s74, s79, 1920\n"
      "s_add_u32 m0, s90, 49536\n"
      "s_nop 0\n"
      "buffer_load_dwordx4 v237, s[8:11], s74 offen offset:0 lds\n"
      "v_mfma_f32_32x32x16_bf16 v[20:35], v[88:91], v[112:115], v[20:35]\n"
      "ds_read_b64_tr_b16 v[72:73], v214 offset:2064\n"
      "ds_read_b64_tr_b16 v[74:75], v214 offset:2576\n"
      "ds_read_b64_tr_b16 v[76:77], v214 offset:4128\n"
      "v_mfma_f32_32x32x16_bf16 v[36:51], v[92:95], v[112:115], v[36:51]\n"
      "ds_read_b64_tr_b16 v[78:79], v214 offset:4640\n"
      "ds_read_b64_tr_b16 v[80:81], v213 offset:0\n"
      "ds_read_b64_tr_b16 v[82:83], v212 offset:0\n"
      "buffer_load_dwordx4 v236, s[8:11], s74 offen offset:1024 lds\n"
      "v_mfma_f32_32x32x16_bf16 v[52:67], v[96:99], v[112:115], v[52:67]\n"
      "s_waitcnt lgkmcnt(6)\n"
      "v_mfma_f32_32x32x16_bf16 v[4:19], v[68:71], v[100:103], v[4:19]\n"
      "ds_read_b128 v[104:107], v211 offset:0\n"
      "ds_read_b64_tr_b16 v[84:85], v206 offset:0\n"
      "ds_read_b64_tr_b16 v[86:87], v206 offset:512\n"
      "buffer_load_dwordx4 v235, s[8:11], s74 offen offset:2064 lds\n"
      "s_waitcnt lgkmcnt(7)\n"
      "v_mfma_f32_32x32x16_bf16 v[20:35], v[72:75], v[100:103], v[20:35]\n"
      "ds_read_b64_tr_b16 v[88:89], v206 offset:2064\n"
      "ds_read_b64_tr_b16 v[90:91], v206 offset:2576\n"
      "ds_read_b64_tr_b16 v[92:93], v206 offset:4128\n"
      "s_waitcnt lgkmcnt(8)\n"
      "v_mfma_f32_32x32x16_bf16 v[36:51], v[76:79], v[100:103], v[36:51]\n"
      "ds_read_b64_tr_b16 v[94:95], v206 offset:4640\n"
      "ds_read_b64_tr_b16 v[96:97], v205 offset:0\n"
      "ds_read_b64_tr_b16 v[98:99], v204 offset:0\n"
      "buffer_load_dwordx4 v234, s[8:11], s74 offen offset:3088 lds\n"
      "s_add_u32 m0, s90, 53664\n"
      "s_waitcnt lgkmcnt(9)\n"
      "v_mfma_f32_32x32x16_bf16 v[52:67], v[80:83], v[100:103], v[52:67]\n"
      "s_waitcnt lgkmcnt(6)\n"
      "v_mfma_f32_32x32x16_bf16 v[4:19], v[84:87], v[104:107], v[4:19]\n"
      "ds_read_b128 v[108:111], v203 offset:0\n"
      "ds_read_b64_tr_b16 v[68:69], v198 offset:0\n"
      "ds_read_b64_tr_b16 v[70:71], v198 offset:512\n"
      "buffer_load_dwordx4 v233, s[8:11], s74 offen offset:0 lds\n"
      "s_waitcnt lgkmcnt(7)\n"
      "v_mfma_f32_32x32x16_bf16 v[20:35], v[88:91], v[104:107], v[20:35]\n"
      "ds_read_b64_tr_b16 v[72:73], v198 offset:2064\n"
      "ds_read_b64_tr_b16 v[74:75], v198 offset:2576\n"
      "ds_read_b64_tr_b16 v[76:77], v198 offset:4128\n"
      "s_waitcnt lgkmcnt(8)\n"
      "v_mfma_f32_32x32x16_bf16 v[36:51], v[92:95], v[104:107], v[36:51]\n"
      "ds_read_b64_tr_b16 v[78:79], v198 offset:4640\n"
      "ds_read_b64_tr_b16 v[80:81], v197 offset:0\n"
      "ds_read_b64_tr_b16 v[82:83], v196 offset:0\n"
      "buffer_load_dwordx4 v232, s[8:11], s74 offen offset:1024 lds\n"
      "s_add_u32 m0, s98, 32768\n"
      "s_waitcnt lgkmcnt(9)\n"
      "v_mfma_f32_32x32x16_bf16 v[52:67], v[96:99], v[104:107], v[52:67]\n"
      "s_waitcnt lgkmcnt(6)\n"
      "v_mfma_f32_32x32x16_bf16 v[4:19], v[68:71], v[108:111], v[4:19]\n"
      "ds_read_b128 v[112:115], v195 offset:0\n"
      "ds_read_b64_tr_b16 v[84:85], v190 offset:0\n"
      "ds_read_b64_tr_b16 v[86:87], v190 offset:512\n"
      "buffer_load_dwordx4 v2, s[4:7], s79 offen offset:0 lds\n"
      "s_waitcnt lgkmcnt(7)\n"
      "v_mfma_f32_32x32x16_bf16 v[20:35], v[72:75], v[108:111], v[20:35]\n"
      "ds_read_b64_tr_b16 v[88:89], v190 offset:2064\n"
      "ds_read_b64_tr_b16 v[90:91], v190 offset:2576\n"
      "ds_read_b64_tr_b16 v[92:93], v190 offset:4128\n"
      "s_waitcnt lgkmcnt(8)\n"
      "v_mfma_f32_32x32x16_bf16 v[36:51], v[76:79], v[108:111], v[36:51]\n"
      "ds_read_b64_tr_b16 v[94:95], v190 offset:4640\n"
      "ds_read_b64_tr_b16 v[96:97], v189 offset:0\n"
      "ds_read_b64_tr_b16 v[98:99], v188 offset:0\n"
      "buffer_load_dwordx4 v249, s[4:7], s79 offen offset:1024 lds\n"
      "s_waitcnt lgkmcnt(9)\n"
      "v_mfma_f32_32x32x16_bf16 v[52:67], v[80:83], v[108:111], v[52:67]\n"
      "s_waitcnt lgkmcnt(0)\n"
      "v_dot2_f32_bf16 v116, v84, s34, v116\n"
      "v_dot2_f32_bf16 v120, v100, v100, v120\n"
      "v_dot2_f32_bf16 v117, v88, s34, v117\n"
      "v_dot2_f32_bf16 v121, v104, v104, v121\n"
      "v_dot2_f32_bf16 v118, v92, s34, v118\n"
      "v_dot2_f32_bf16 v122, v108, v108, v122\n"
      "v_dot2_f32_bf16 v119, v96, s34, v119\n"
      "v_dot2_f32_bf16 v123, v112, v112, v123\n"
      "buffer_load_dwordx4 v246, s[4:7], s79 offen offset:2048 lds\n"
      "v_dot2_f32_bf16 v116, v85, s34, v116\n"
      "v_dot2_f32_bf16 v120, v101, v101, v120\n"
      "v_dot2_f32_bf16 v117, v89, s34, v117\n"
      "v_dot2_f32_bf16 v121, v105, v105, v121\n"
      "v_dot2_f32_bf16 v118, v93, s34, v118\n"
      "v_dot2_f32_bf16 v122, v109, v109, v122\n"
      "v_dot2_f32_bf16 v119, v97, s34, v119\n"
      "v_dot2_f32_bf16 v123, v113, v113, v123\n"
      "v_dot2_f32_bf16 v116, v86, s34, v116\n"
      "v_dot2_f32_bf16 v120, v102, v102, v120\n"
      "v_dot2_f32_bf16 v117, v90, s34, v117\n"
      "v_dot2_f32_bf16 v121, v106, v106, v121\n"
      "v_dot2_f32_bf16 v118, v94, s34, v118\n"
      "v_dot2_f32_bf16 v122, v110, v110, v122\n"
      "v_dot2_f32_bf16 v119, v98, s34, v119\n"
      "v_dot2_f32_bf16 v123, v114, v114, v123\n"
      "buffer_load_dwordx4 v244, s[4:7], s79 offen offset:3072 lds\n"
      "s_add_u32 s79, s79, 128\n"
      "s_cmp_eq_u32 s79, 1280\n"
      "s_cselect_b32 s79, 0, s79\n"
      "v_dot2_f32_bf16 v116, v87, s34, v116\n"
      "v_dot2_f32_bf16 v120, v103, v103, v120\n"
      "v_dot2_f32_bf16 v117, v91, s34, v117\n"
      "v_dot2_f32_bf16 v121, v107, v107, v121\n"
      "v_dot2_f32_bf16 v118, v95, s34, v118\n"
      "v_dot2_f32_bf16 v122, v111, v111, v122\n"
      "v_dot2_f32_bf16 v119, v99, s34, v119\n"
      "v_dot2_f32_bf16 v123, v115, v115, v123\n"
      "buffer_load_dword v247, v247, s[12:15], 0 offen\n"
      "buffer_load_dword v170, v170, s[20:23], 0 offen\n"
      "buffer_load_dword v250, v250, s[36:39], 0 offen sc1\n"
      "s_waitcnt vmcnt(13)\n"
      "s_waitcnt lgkmcnt(0)\n"
      "s_barrier\n"
      "v_mfma_f32_32x32x16_bf16 v[4:19], v[84:87], v[112:115], v[4:19]\n"
      "ds_read_b128 v[100:103], v222 offset:16384\n"
      "ds_read_b64_tr_b16 v[68:69], v214 offset:24768\n"
      "ds_read_b64_tr_b16 v[70:71], v214 offset:25280\n"
      "s_mul_i32 s74, s79, 1920\n"
      "s_add_u32 m0, s90, 0\n"
      "s_nop 0\n"
      "buffer_load_dwordx4 v237, s[8:11], s74 offen offset:0 lds\n"
      "v_mfma_f32_32x32x16_bf16 v[20:35], v[88:91], v[112:115], v[20:35]\n"
      "ds_read_b64_tr_b16 v[72:73], v214 offset:26832\n"
      "ds_read_b64_tr_b16 v[74:75], v214 offset:27344\n"
      "ds_read_b64_tr_b16 v[76:77], v214 offset:28896\n"
      "v_mfma_f32_32x32x16_bf16 v[36:51], v[92:95], v[112:115], v[36:51]\n"
      "ds_read_b64_tr_b16 v[78:79], v214 offset:29408\n"
      "ds_read_b64_tr_b16 v[80:81], v213 offset:24768\n"
      "ds_read_b64_tr_b16 v[82:83], v212 offset:24768\n"
      "buffer_load_dwordx4 v236, s[8:11], s74 offen offset:1024 lds\n"
      "v_mfma_f32_32x32x16_bf16 v[52:67], v[96:99], v[112:115], v[52:67]\n"
      "s_waitcnt lgkmcnt(6)\n"
      "v_mfma_f32_32x32x16_bf16 v[4:19], v[68:71], v[100:103], v[4:19]\n"
      "ds_read_b128 v[104:107], v211 offset:16384\n"
      "ds_read_b64_tr_b16 v[84:85], v206 offset:24768\n"
      "ds_read_b64_tr_b16 v[86:87], v206 offset:25280\n"
      "buffer_load_dwordx4 v235, s[8:11], s74 offen offset:2064 lds\n"
      "s_waitcnt lgkmcnt(7)\n"
      "v_mfma_f32_32x32x16_bf16 v[20:35], v[72:75], v[100:103], v[20:35]\n"
      "ds_read_b64_tr_b16 v[88:89], v206 offset:26832\n"
      "ds_read_b64_tr_b16 v[90:91], v206 offset:27344\n"
      "ds_read_b64_tr_b16 v[92:93], v206 offset:28896\n"
      "s_waitcnt lgkmcnt(8)\n"
      "v_mfma_f32_32x32x16_bf16 v[36:51], v[76:79], v[100:103], v[36:51]\n"
      "ds_read_b64_tr_b16 v[94:95], v206 offset:29408\n"
      "ds_read_b64_tr_b16 v[96:97], v205 offset:24768\n"
      "ds_read_b64_tr_b16 v[98:99], v204 offset:24768\n"
      "buffer_load_dwordx4 v234, s[8:11], s74 offen offset:3088 lds\n"
      "s_add_u32 m0, s90, 4128\n"
      "s_waitcnt lgkmcnt(9)\n"
      "v_mfma_f32_32x32x16_bf16 v[52:67], v[80:83], v[100:103], v[52:67]\n"
      "s_waitcnt lgkmcnt(6)\n"
      "v_mfma_f32_32x32x16_bf16 v[4:19], v[84:87], v[104:107], v[4:19]\n"
      "ds_read_b128 v[108:111], v203 offset:16384\n"
      "ds_read_b64_tr_b16 v[68:69], v198 offset:24768\n"
      "ds_read_b64_tr_b16 v[70:71], v198 offset:25280\n"
      "buffer_load_dwordx4 v233, s[8:11], s74 offen offset:0 lds\n"
      "s_waitcnt lgkmcnt(7)\n"
      "v_mfma_f32_32x32x16_bf16 v[20:35], v[88:91], v[104:107], v[20:35]\n"
      "ds_read_b64_tr_b16 v[72:73], v198 offset:26832\n"
      "ds_read_b64_tr_b16 v[74:75], v198 offset:27344\n"
      "ds_read_b64_tr_b16 v[76:77], v198 offset:28896\n"
      "s_waitcnt lgkmcnt(8)\n"
      "v_mfma_f32_32x32x16_bf16 v[36:51], v[92:95], v[104:107], v[36:51]\n"
      "ds_read_b64_tr_b16 v[78:79], v198 offset:29408\n"
      "ds_read_b64_tr_b16 v[80:81], v197 offset:24768\n"
      "ds_read_b64_tr_b16 v[82:83], v196 offset:24768\n"
      "buffer_load_dwordx4 v232, s[8:11], s74 offen offset:1024 lds\n"
      "s_add_u32 m0, s98, 0\n"
      "s_waitcnt lgkmcnt(9)\n"
      "v_mfma_f32_32x32x16_bf16 v[52:67], v[96:99], v[104:107], v[52:67]\n"
      "s_waitcnt lgkmcnt(6)\n"
      "v_mfma_f32_32x32x16_bf16 v[4:19], v[68:71], v[108:111], v[4:19]\n"
      "ds_read_b128 v[112:115], v195 offset:16384\n"
      "ds_read_b64_tr_b16 v[84:85], v190 offset:24768\n"
      "ds_read_b64_tr_b16 v[86:87], v190 offset:25280\n"
      "buffer_load_dwordx4 v2, s[4:7], s79 offen offset:0 lds\n"
      "s_waitcnt lgkmcnt(7)\n"
      "v_mfma_f32_32x32x16_bf16 v[20:35], v[72:75], v[108:111], v[20:35]\n"
      "ds_read_b64_tr_b16 v[88:89], v190 offset:26832\n"
      "ds_read_b64_tr_b16 v[90:91], v190 offset:27344\n"
      "ds_read_b64_tr_b16 v[92:93], v190 offset:28896\n"
      "s_waitcnt lgkmcnt(8)\n"
      "v_mfma_f32_32x32x16_bf16 v[36:51], v[76:79], v[108:111], v[36:51]\n"
      "ds_read_b64_tr_b16 v[94:95], v190 offset:29408\n"
      "ds_read_b64_tr_b16 v[96:97], v189 offset:24768\n"
      "ds_read_b64_tr_b16 v[98:99], v188 offset:24768\n"
      "buffer_load_dwordx4 v249, s[4:7], s79 offen offset:1024 lds\n"
      "s_waitcnt lgkmcnt(9)\n"
      "v_mfma_f32_32x32x16_bf16 v[52:67], v[80:83], v[108:111], v[52:67]\n"
      "s_waitcnt lgkmcnt(0)\n"
      "v_dot2_f32_bf16 v116, v84, s34, v116\n"
      "v_dot2_f32_bf16 v120, v100, v100, v120\n"
      "v_dot2_f32_bf16 v117, v88, s34, v117\n"
      "v_dot2_f32_bf16 v121, v104, v104, v121\n"
      "v_dot2_f32_bf16 v118, v92, s34, v118\n"
      "v_dot2_f32_bf16 v122, v108, v108, v122\n"
      "v_dot2_f32_bf16 v119, v96, s34, v119\n"
      "v_dot2_f32_bf16 v123, v112, v112, v123\n"
      "buffer_load_dwordx4 v246, s[4:7], s79 offen offset:2048 lds\n"
      "v_dot2_f32_bf16 v116, v85, s34, v116\n"
      "v_dot2_f32_bf16 v120, v101, v101, v120\n"
      "v_dot2_f32_bf16 v117, v89, s34, v117\n"
      "v_dot2_f32_bf16 v121, v105, v105, v121\n"
      "v_dot2_f32_bf16 v118, v93, s34, v118\n"
      "v_dot2_f32_bf16 v122, v109, v109, v122\n"
      "v_dot2_f32_bf16 v119, v97, s34, v119\n"
      "v_dot2_f32_bf16 v123, v113, v113, v123\n"
      "v_dot2_f32_bf16 v116, v86, s34, v116\n"
      "v_dot2_f32_bf16 v120, v102, v102, v120\n"
      "v_dot2_f32_bf16 v117, v90, s34, v117\n"
      "v_dot2_f32_bf16 v121, v106, v106, v121\n"
      "v_dot2_f32_bf16 v118, v94, s34, v118\n"
      "v_dot2_f32_bf16 v122, v110, v110, v122\n"
      "v_dot2_f32_bf16 v119, v98, s34, v119\n"
      "v_dot2_f32_bf16 v123, v114, v114, v123\n"
      "buffer_load_dwordx4 v244, s[4:7], s79 offen offset:3072 lds\n"
      "s_add_u32 s79, s79, 128\n"
      "s_cmp_eq_u32 s79, 1280\n"
      "s_cselect_b32 s79, 0, s79\n"
      "v_dot2_f32_bf16 v116, v87, s34, v116\n"
      "v_dot2_f32_bf16 v120, v103, v103, v120\n"
      "v_dot2_f32_bf16 v117, v91, s34, v117\n"
      "v_dot2_f32_bf16 v121, v107, v107, v121\n"
      "v_dot2_f32_bf16 v118, v95, s34, v118\n"
      "v_dot2_f32_bf16 v122, v111, v111, v122\n"
      "v_dot2_f32_bf16 v119, v99, s34, v119\n"
      "v_dot2_f32_bf16 v123, v115, v115, v123\n"
      "s_waitcnt vmcnt(13)\n"
      "s_waitcnt lgkmcnt(0)\n"
      "s_barrier\n"
      "v_mfma_f32_32x32x16_bf16 v[4:19], v[84:87], v[112:115], v[4:19]\n"
      "ds_read_b128 v[100:103], v222 offset:32768\n"
      "ds_read_b64_tr_b16 v[68:69], v214 offset:49536\n"
      "ds_read_b64_tr_b16 v[70:71], v214 offset:50048\n"
      "s_add_u32 m0, s85, 81920\n"
      "s_add_u32 s94, s81, 0\n"
      "buffer_load_dwordx4 v175, s[16:19], s94 offen offset:0 lds\n"
      "v_mfma_f32_32x32x16_bf16 v[20:35], v[88:91], v[112:115], v[20:35]\n"
      "ds_read_b64_tr_b16 v[72:73], v214 offset:51600\n"
      "ds_read_b64_tr_b16 v[74:75], v214 offset:52112\n"
      "ds_read_b64_tr_b16 v[76:77], v214 offset:53664\n"
      "v_mfma_f32_32x32x16_bf16 v[36:51], v[92:95], v[112:115], v[36:51]\n"
      "ds_read_b64_tr_b16 v[78:79], v214 offset:54176\n"
      "ds_read_b64_tr_b16 v[80:81], v213 offset:49536\n"
      "ds_read_b64_tr_b16 v[82:83], v212 offset:49536\n"
      "buffer_load_dwordx4 v238, s[16:19], s94 offen offset:1024 lds\n"
      "v_mfma_f32_32x32x16_bf16 v[52:67], v[96:99], v[112:115], v[52:67]\n"
      "s_waitcnt lgkmcnt(6)\n"
      "v_mfma_f32_32x32x16_bf16 v[4:19], v[68:71], v[100:103], v[4:19]\n"
      "ds_read_b128 v[104:107], v211 offset:32768\n"
      "ds_read_b64_tr_b16 v[84:85], v206 offset:49536\n"
      "ds_read_b64_tr_b16 v[86:87], v206 offset:50048\n"
      "buffer_load_dwordx4 v221, s[16:19], s94 offen offset:2048 lds\n"
      "s_waitcnt lgkmcnt(7)\n"
      "v_mfma_f32_32x32x16_bf16 v[20:35], v[72:75], v[100:103], v[20:35]\n"
      "ds_read_b64_tr_b16 v[88:89], v206 offset:51600\n"
      "ds_read_b64_tr_b16 v[90:91], v206 offset:52112\n"
      "ds_read_b64_tr_b16 v[92:93], v206 offset:53664\n"
      "s_waitcnt lgkmcnt(8)\n"
      "v_mfma_f32_32x32x16_bf16 v[36:51], v[76:79], v[100:103], v[36:51]\n"
      "ds_read_b64_tr_b16 v[94:95], v206 offset:54176\n"
      "ds_read_b64_tr_b16 v[96:97], v205 offset:49536\n"
      "ds_read_b64_tr_b16 v[98:99], v204 offset:49536\n"
      "buffer_load_dwordx4 v179, s[16:19], s94 offen offset:3072 lds\n"
      "s_add_u32 m0, s85, 86016\n"
      "s_waitcnt lgkmcnt(9)\n"
      "v_mfma_f32_32x32x16_bf16 v[52:67], v[80:83], v[100:103], v[52:67]\n"
      "s_waitcnt lgkmcnt(6)\n"
      "v_mfma_f32_32x32x16_bf16 v[4:19], v[84:87], v[104:107], v[4:19]\n"
      "ds_read_b128 v[108:111], v203 offset:32768\n"
      "ds_read_b64_tr_b16 v[68:69], v198 offset:49536\n"
      "ds_read_b64_tr_b16 v[70:71], v198 offset:50048\n"
      "buffer_load_dwordx4 v209, s[16:19], s94 offen offset:0 lds\n"
      "s_waitcnt lgkmcnt(7)\n"
      "v_mfma_f32_32x32x16_bf16 v[20:35], v[88:91], v[104:107], v[20:35]\n"
      "ds_read_b64_tr_b16 v[72:73], v198 offset:51600\n"
      "ds_read_b64_tr_b16 v[74:75], v198 offset:52112\n"
      "ds_read_b64_tr_b16 v[76:77], v198 offset:53664\n"
      "s_waitcnt lgkmcnt(8)\n"
      "v_mfma_f32_32x32x16_bf16 v[36:51], v[92:95], v[104:107], v[36:51]\n"
      "ds_read_b64_tr_b16 v[78:79], v198 offset:54176\n"
      "ds_read_b64_tr_b16 v[80:81], v197 offset:49536\n"
      "ds_read_b64_tr_b16 v[82:83], v196 offset:49536\n"
      "s_waitcnt lgkmcnt(9)\n"
      "v_mfma_f32_32x32x16_bf16 v[52:67], v[96:99], v[104:107], v[52:67]\n"
      "s_waitcnt lgkmcnt(6)\n"
      "v_mfma_f32_32x32x16_bf16 v[4:19], v[68:71], v[108:111], v[4:19]\n"
      "ds_read_b128 v[112:115], v195 offset:32768\n"
      "ds_read_b64_tr_b16 v[84:85], v190 offset:49536\n"
      "ds_read_b64_tr_b16 v[86:87], v190 offset:50048\n"
      "s_waitcnt lgkmcnt(7)\n"
      "v_mfma_f32_32x32x16_bf16 v[20:35], v[72:75], v[108:111], v[20:35]\n"
      "ds_read_b64_tr_b16 v[88:89], v190 offset:51600\n"
      "ds_read_b64_tr_b16 v[90:91], v190 offset:52112\n"
      "ds_read_b64_tr_b16 v[92:93], v190 offset:53664\n"
      "s_waitcnt lgkmcnt(8)\n"
      "v_mfma_f32_32x32x16_bf16 v[36:51], v[76:79], v[108:111], v[36:51]\n"
      "ds_read_b64_tr_b16 v[94:95], v190 offset:54176\n"
      "ds_read_b64_tr_b16 v[96:97], v189 offset:49536\n"
      "ds_read_b64_tr_b16 v[98:99], v188 offset:49536\n"
      "s_waitcnt lgkmcnt(9)\n"
      "v_mfma_f32_32x32x16_bf16 v[52:67], v[80:83], v[108:111], v[52:67]\n"
      "s_waitcnt lgkmcnt(0)\n"
      "v_dot2_f32_bf16 v116, v84, s34, v116\n"
      "v_dot2_f32_bf16 v120, v100, v100, v120\n"
      "v_dot2_f32_bf16 v117, v88, s34, v117\n"
      "v_dot2_f32_bf16 v121, v104, v104, v121\n"
      "v_dot2_f32_bf16 v118, v92, s34, v118\n"
      "v_dot2_f32_bf16 v122, v108, v108, v122\n"
      "v_dot2_f32_bf16 v119, v96, s34, v119\n"
      "v_dot2_f32_bf16 v123, v112, v112, v123\n"
      "v_dot2_f32_bf16 v116, v85, s34, v116\n"
      "v_dot2_f32_bf16 v120, v101, v101, v120\n"
      "v_dot2_f32_bf16 v117, v89, s34, v117\n"
      "v_dot2_f32_bf16 v121, v105, v105, v121\n"
      "v_dot2_f32_bf16 v118, v93, s34, v118\n"
      "v_dot2_f32_bf16 v122, v109, v109, v122\n"
      "v_dot2_f32_bf16 v119, v97, s34, v119\n"
      "v_dot2_f32_bf16 v123, v113, v113, v123\n"
      "v_dot2_f32_bf16 v116, v86, s34, v116\n"
      "v_dot2_f32_bf16 v120, v102, v102, v120\n"
      "v_dot2_f32_bf16 v117, v90, s34, v117\n"
      "v_dot2_f32_bf16 v121, v106, v106, v121\n"
      "v_dot2_f32_bf16 v118, v94, s34, v118\n"
      "v_dot2_f32_bf16 v122, v110, v110, v122\n"
      "v_dot2_f32_bf16 v119, v98, s34, v119\n"
      "v_dot2_f32_bf16 v123, v114, v114, v123\n"
      "v_dot2_f32_bf16 v116, v87, s34, v116\n"
      "v_dot2_f32_bf16 v120, v103, v103, v120\n"
      "v_dot2_f32_bf16 v117, v91, s34, v117\n"
      "v_dot2_f32_bf16 v121, v107, v107, v121\n"
      "v_dot2_f32_bf16 v118, v95, s34, v118\n"
      "v_dot2_f32_bf16 v122, v111, v111, v122\n"
      "v_dot2_f32_bf16 v119, v99, s34, v119\n"
      "v_dot2_f32_bf16 v123, v115, v115, v123\n"
      "s_waitcnt vmcnt(5)\n"
      "s_waitcnt lgkmcnt(0)\n"
      "s_barrier\n"
      "v_mfma_f32_32x32x16_bf16 v[4:19], v[84:87], v[112:115], v[4:19]\n"
      "ds_read_b128 v[100:103], v222 offset:0\n"
      "ds_read_b64_tr_b16 v[68:69], v214 offset:0\n"
      "ds_read_b64_tr_b16 v[70:71], v214 offset:512\n"
      "s_add_u32 m0, s77, 143360\n"
      "s_nop 0\n"
      "buffer_load_dwordx4 v245, s[4:7], 0 offen offset:0 lds\n"
      "v_mfma_f32_32x32x16_bf16 v[20:35], v[88:91], v[112:115], v[20:35]\n"
      "ds_read_b64_tr_b16 v[72:73], v214 offset:2064\n"
      "ds_read_b64_tr_b16 v[74:75], v214 offset:2576\n"
      "ds_read_b64_tr_b16 v[76:77], v214 offset:4128\n"
      "v_mfma_f32_32x32x16_bf16 v[36:51], v[92:95], v[112:115], v[36:51]\n"
      "ds_read_b64_tr_b16 v[78:79], v214 offset:4640\n"
      "ds_read_b64_tr_b16 v[80:81], v213 offset:0\n"
      "ds_read_b64_tr_b16 v[82:83], v212 offset:0\n"
      "buffer_load_dwordx4 v210, s[4:7], 0 offen offset:1024 lds\n"
      "v_mfma_f32_32x32x16_bf16 v[52:67], v[96:99], v[112:115], v[52:67]\n"
      "s_waitcnt lgkmcnt(6)\n"
      "v_mfma_f32_32x32x16_bf16 v[4:19], v[68:71], v[100:103], v[4:19]\n"
      "ds_read_b128 v[104:107], v211 offset:0\n"
      "ds_read_b64_tr_b16 v[84:85], v206 offset:0\n"
      "ds_read_b64_tr_b16 v[86:87], v206 offset:512\n"
      "buffer_load_dwordx4 v192, s[4:7], 0 offen offset:2048 lds\n"
      "s_waitcnt lgkmcnt(7)\n"
      "v_mfma_f32_32x32x16_bf16 v[20:35], v[72:75], v[100:103], v[20:35]\n"
      "ds_read_b64_tr_b16 v[88:89], v206 offset:2064\n"
      "ds_read_b64_tr_b16 v[90:91], v206 offset:2576\n"
      "ds_read_b64_tr_b16 v[92:93], v206 offset:4128\n"
      "s_waitcnt lgkmcnt(8)\n"
      "v_mfma_f32_32x32x16_bf16 v[36:51], v[76:79], v[100:103], v[36:51]\n"
      "ds_read_b64_tr_b16 v[94:95], v206 offset:4640\n"
      "ds_read_b64_tr_b16 v[96:97], v205 offset:0\n"
      "ds_read_b64_tr_b16 v[98:99], v204 offset:0\n"
      "s_waitcnt lgkmcnt(9)\n"
      "v_mfma_f32_32x32x16_bf16 v[52:67], v[80:83], v[100:103], v[52:67]\n"
      "s_waitcnt lgkmcnt(6)\n"
      "v_mfma_f32_32x32x16_bf16 v[4:19], v[84:87], v[104:107], v[4:19]\n"
      "ds_read_b128 v[108:111], v203 offset:0\n"
      "ds_read_b64_tr_b16 v[68:69], v198 offset:0\n"
      "ds_read_b64_tr_b16 v[70:71], v198 offset:512\n"
      "s_waitcnt lgkmcnt(7)\n"
      "v_mfma_f32_32x32x16_bf16 v[20:35], v[88:91], v[104:107], v[20:35]\n"
      "ds_read_b64_tr_b16 v[72:73], v198 offset:2064\n"
      "ds_read_b64_tr_b16 v[74:75], v198 offset:2576\n"
      "ds_read_b64_tr_b16 v[76:77], v198 offset:4128\n"
      "s_waitcnt lgkmcnt(8)\n"
      "v_mfma_f32_32x32x16_bf16 v[36:51], v[92:95], v[104:107], v[36:51]\n"
      "ds_read_b64_tr_b16 v[78:79], v198 offset:4640\n"
      "ds_read_b64_tr_b16 v[80:81], v197 offset:0\n"
      "ds_read_b64_tr_b16 v[82:83], v196 offset:0\n"
      "s_waitcnt lgkmcnt(9)\n"
      "v_mfma_f32_32x32x16_bf16 v[52:67], v[96:99], v[104:107], v[52:67]\n"
      "s_waitcnt lgkmcnt(6)\n"
      "v_mfma_f32_32x32x16_bf16 v[4:19], v[68:71], v[108:111], v[4:19]\n"
      "ds_read_b128 v[112:115], v195 offset:0\n"
      "ds_read_b64_tr_b16 v[84:85], v190 offset:0\n"
      "ds_read_b64_tr_b16 v[86:87], v190 offset:512\n"
      "s_waitcnt lgkmcnt(7)\n"
      "v_mfma_f32_32x32x16_bf16 v[20:35], v[72:75], v[108:111], v[20:35]\n"
      "ds_read_b64_tr_b16 v[88:89], v190 offset:2064\n"
      "ds_read_b64_tr_b16 v[90:91], v190 offset:2576\n"
      "ds_read_b64_tr_b16 v[92:93], v190 offset:4128\n"
      "s_waitcnt lgkmcnt(8)\n"
      "v_mfma_f32_32x32x16_bf16 v[36:51], v[76:79], v[108:111], v[36:51]\n"
      "ds_read_b64_tr_b16 v[94:95], v190 offset:4640\n"
      "ds_read_b64_tr_b16 v[96:97], v189 offset:0\n"
      "ds_read_b64_tr_b16 v[98:99], v188 offset:0\n"
      "s_waitcnt lgkmcnt(9)\n"
      "v_mfma_f32_32x32x16_bf16 v[52:67], v[80:83], v[108:111], v[52:67]\n"
      "s_waitcnt lgkmcnt(0)\n"
      "v_dot2_f32_bf16 v116, v84, s34, v116\n"
      "v_dot2_f32_bf16 v120, v100, v100, v120\n"
      "v_dot2_f32_bf16 v117, v88, s34, v117\n"
      "v_dot2_f32_bf16 v121, v104, v104, v121\n"
      "v_dot2_f32_bf16 v118, v92, s34, v118\n"
      "v_dot2_f32_bf16 v122, v108, v108, v122\n"
      "v_dot2_f32_bf16 v119, v96, s34, v119\n"
      "v_dot2_f32_bf16 v123, v112, v112, v123\n"
      "v_dot2_f32_bf16 v116, v85, s34, v116\n"
      "v_dot2_f32_bf16 v120, v101, v101, v120\n"
      "v_dot2_f32_bf16 v117, v89, s34, v117\n"
      "v_dot2_f32_bf16 v121, v105, v105, v121\n"
      "v_dot2_f32_bf16 v118, v93, s34, v118\n"
      "v_dot2_f32_bf16 v122, v109, v109, v122\n"
      "v_dot2_f32_bf16 v119, v97, s34, v119\n"
      "v_dot2_f32_bf16 v123, v113, v113, v123\n"
      "v_dot2_f32_bf16 v116, v86, s34, v116\n"
      "v_dot2_f32_bf16 v120, v102, v102, v120\n"
      "v_dot2_f32_bf16 v117, v90, s34, v117\n"
      "v_dot2_f32_bf16 v121, v106, v106, v121\n"
      "v_dot2_f32_bf16 v118, v94, s34, v118\n"
      "v_dot2_f32_bf16 v122, v110, v110, v122\n"
      "v_dot2_f32_bf16 v119, v98, s34, v119\n"
      "v_dot2_f32_bf16 v123, v114, v114, v123\n"
      "v_dot2_f32_bf16 v116, v87, s34, v116\n"
      "v_dot2_f32_bf16 v120, v103, v103, v120\n"
      "v_dot2_f32_bf16 v117, v91, s34, v117\n"
      "v_dot2_f32_bf16 v121, v107, v107, v121\n"
      "v_dot2_f32_bf16 v118, v95, s34, v118\n"
      "v_dot2_f32_bf16 v122, v111, v111, v122\n"
      "v_dot2_f32_bf16 v119, v99, s34, v119\n"
      "v_dot2_f32_bf16 v123, v115, v115, v123\n"
      "v_mfma_f32_32x32x16_bf16 v[52:67], v[96:99], v[112:115], v[52:67]\n"
      "v_add_f32 v120, v120, v121\n"
      "s_nop 0\n"
      "v_add_f32 v122, v122, v123\n"
      "v_permlane32_swap_b32 v116, v117\n"
      "v_permlane32_swap_b32 v118, v119\n"
      "v_add_f32 v120, v120, v122\n"
      "v_mfma_f32_32x32x16_bf16 v[4:19], v[84:87], v[112:115], v[4:19]\n"
      "v_add_f32 v116, v116, v117\n"
      "v_add_f32 v118, v118, v119\n"
      "v_mov_b32 v121, v120\n"
      "v_cvt_pk_bf16_f32 v180, v116, v116\n"
      "v_cvt_pk_bf16_f32 v181, v118, v118\n"
      "v_mfma_f32_32x32x16_bf16 v[20:35], v[88:91], v[112:115], v[20:35]\n"
      "v_permlane32_swap_b32 v120, v121\n"
      "v_and_b32 v180, 0xffff0000, v180\n"
      "v_and_b32 v181, 0xffff0000, v181\n"
      "v_sub_f32 v180, v116, v180\n"
      "v_sub_f32 v181, v118, v181\n"
      "v_mfma_f32_32x32x16_bf16 v[36:51], v[92:95], v[112:115], v[36:51]\n"
      "v_cvt_pk_bf16_f32 v180, v116, v180\n"
      "v_cvt_pk_bf16_f32 v181, v118, v181\n"
      "ds_write_b32 v220, v180\n"
      "ds_write_b32 v220, v181 offset:1024\n"
      "v_add_f32 v120, v120, v121\n"
      "s_waitcnt lgkmcnt(0)\n"
      "s_barrier\n"
      "s_add_u32 m0, s85, 40960\n"
      "s_add_u32 s94, s78, 327680\n"
      "buffer_load_dwordx4 v207, s[12:15], s94 offen offset:0 lds\n"
      "buffer_load_dwordx4 v199, s[12:15], s94 offen offset:1024 lds\n"
      "ds_read_b128 v[68:71], v187 offset:0\n"
      "ds_read_b128 v[80:83], v187 offset:1536\n"
      "ds_read_b128 v[72:75], v187 offset:512\n"
      "ds_read_b128 v[76:79], v187 offset:1024\n"
      "s_mov_b32 s74, 0\n"
      "s_mov_b32 s75, -1\n"
      "v_mul_f32 v180, 0xbacccccd, v64\n"
      "v_cvt_pk_bf16_f32 v181, v180, v180\n"
      "v_mul_f32 v120, 0x3acccccd, v120\n"
      "v_and_b32 v181, 0xffff0000, v181\n"
      "v_fma_f32 v120, -v180, v180, v120\n"
      "v_sub_f32 v181, v180, v181\n"
      "v_max_f32 v120, 0, v120\n"
      "v_cndmask_b32 v181, v180, v181, s[74:75]\n"
      "v_add_f32 v120, 0x3727c5ac, v120\n"
      "v_cvt_pk_bf16_f32 v100, v181, v181\n"
      "v_cvt_pk_bf16_f32 v101, v181, v181\n"
      "v_cvt_pk_bf16_f32 v102, v181, v181\n"
      "v_cvt_pk_bf16_f32 v103, v181, v181\n"
      "v_rsq_f32 v236, v120\n"
      "s_waitcnt lgkmcnt(3)\n"
      "v_mfma_f32_32x32x16_bf16 v[4:19], v[68:71], v[100:103], v[4:19]\n"
      "s_waitcnt lgkmcnt(2)\n"
      "v_mfma_f32_32x32x16_bf16 v[52:67], v[80:83], v[100:103], v[52:67]\n"
      "buffer_load_dwordx4 v194, s[12:15], s94 offen offset:2048 lds\n"
      "s_waitcnt lgkmcnt(1)\n"
      "v_mfma_f32_32x32x16_bf16 v[20:35], v[72:75], v[100:103], v[20:35]\n"
      "s_waitcnt lgkmcnt(0)\n"
      "v_mfma_f32_32x32x16_bf16 v[36:51], v[76:79], v[100:103], v[36:51]\n"
      "v_mul_f32 v204, 0x3fb8aa3b, v236\n"
      "v_mov_b32 v130, 0x3f80\n"
      "v_mov_b32 v131, 0\n"
      "s_nop 1\n"
      "v_cvt_pk_bf16_f32 v132, v4, v5\n"
      "v_cvt_pk_bf16_f32 v133, v6, v7\n"
      "v_cvt_pk_bf16_f32 v134, v8, v9\n"
      "v_cvt_pk_bf16_f32 v135, v10, v11\n"
      "v_cvt_pk_bf16_f32 v136, v12, v13\n"
      "v_cvt_pk_bf16_f32 v137, v14, v15\n"
      "v_cvt_pk_bf16_f32 v138, v16, v17\n"
      "v_cvt_pk_bf16_f32 v139, v18, v19\n"
      "v_cvt_pk_bf16_f32 v140, v52, v53\n"
      "buffer_load_dwordx4 v191, s[12:15], s94 offen offset:3072 lds\n"
      "v_cvt_pk_bf16_f32 v141, v54, v55\n"
      "v_mov_b32 v142, 0\n"
      "v_mov_b32 v143, 0\n"
      "v_mfma_f32_32x32x16_bf16 v[68:83], a[20:23], v[132:135], 0\n"
      "v_mul_f32 v20, v20, v236\n"
      "v_mul_f32 v21, v21, v236\n"
      "v_cvt_pk_bf16_f32 v120, v20, v21\n"
      "v_mul_f32 v22, v22, v236\n"
      "v_mul_f32 v23, v23, v236\n"
      "v_mfma_f32_32x32x16_bf16 v[84:99], a[32:35], v[132:135], 0\n"
      "v_cvt_pk_bf16_f32 v121, v22, v23\n"
      "v_mul_f32 v24, v24, v236\n"
      "v_mul_f32 v25, v25, v236\n"
      "v_cvt_pk_bf16_f32 v122, v24, v25\n"
      "v_mul_f32 v26, v26, v236\n"
      "v_mfma_f32_32x32x16_bf16 v[68:83], a[24:27], v[136:139], v[68:83]\n"
      "s_add_u32 m0, s85, 45056\n"
      "s_nop 0\n"
      "buffer_load_dwordx4 v186, s[12:15], s94 offen offset:0 lds\n"
      "v_mul_f32 v27, v27, v236\n"
      "v_cvt_pk_bf16_f32 v123, v26, v27\n"
      "v_mul_f32 v28, v28, v236\n"
      "v_mul_f32 v29, v29, v236\n"
      "v_cvt_pk_bf16_f32 v124, v28, v29\n"
      "v_mfma_f32_32x32x16_bf16 v[84:99], a[36:39], v[136:139], v[84:99]\n"
      "v_mul_f32 v30, v30, v236\n"
      "v_mul_f32 v31, v31, v236\n"
      "v_cvt_pk_bf16_f32 v125, v30, v31\n"
      "v_mul_f32 v32, v32, v236\n"
      "v_mul_f32 v33, v33, v236\n"
      "v_mfma_f32_32x32x16_bf16 v[68:83], a[28:31], v[140:143], v[68:83]\n"
      "v_cvt_pk_bf16_f32 v126, v32, v33\n"
      "s_add_u32 m0, s85, 61440\n"
      "s_add_u32 s94, s78, 491520\n"
      "buffer_load_dwordx4 v207, s[12:15], s94 offen offset:0 lds\n"
      "v_mul_f32 v34, v34, v236\n"
      "v_mul_f32 v35, v35, v236\n"
      "v_cvt_pk_bf16_f32 v127, v34, v35\n"
      "v_mul_f32 v56, v56, v236\n"
      "v_mfma_f32_32x32x16_bf16 v[84:99], a[40:43], v[140:143], v[84:99]\n"
      "v_mul_f32 v57, v57, v236\n"
      "v_cvt_pk_bf16_f32 v128, v56, v57\n"
      "v_mul_f32 v58, v58, v236\n"
      "v_mul_f32 v59, v59, v236\n"
      "v_cvt_pk_bf16_f32 v129, v58, v59\n"
      "s_waitcnt vmcnt(9)\n"
      "ds_read_b64_tr_b16 a[0:1], v248 offset:20480\n"
      "ds_read_b64_tr_b16 a[2:3], v248 offset:23040\n"
      "ds_read_b64_tr_b16 a[4:5], v248 offset:20512\n"
      "ds_read_b64_tr_b16 a[6:7], v248 offset:23072\n"
      "ds_read_b64_tr_b16 a[8:9], v248 offset:20544\n"
      "ds_read_b64_tr_b16 a[10:11], v248 offset:23104\n"
      "ds_read_b64_tr_b16 a[12:13], v248 offset:20576\n"
      "ds_read_b64_tr_b16 a[14:15], v248 offset:23136\n"
      "ds_read_b64_tr_b16 a[16:17], v248 offset:20608\n"
      "ds_read_b64_tr_b16 a[18:19], v248 offset:23168\n"
      "s_waitcnt lgkmcnt(8)\n"
      "buffer_store_dwordx4 a[0:3], v3, s[36:39], s71 offen offset:0 sc1\n"
      "s_waitcnt lgkmcnt(6)\n"
      "buffer_store_dwordx4 a[4:7], v3, s[36:39], s71 offen offset:1024 sc1\n"
      "s_waitcnt lgkmcnt(4)\n"
      "buffer_store_dwordx4 a[8:11], v3, s[36:39], s71 offen offset:2048 sc1\n"
      "s_waitcnt lgkmcnt(2)\n"
      "buffer_store_dwordx4 a[12:15], v3, s[36:39], s71 offen offset:3072 sc1\n"
      "s_waitcnt lgkmcnt(0)\n"
      "buffer_store_dwordx4 a[16:19], v3, s[36:39], s66 offen offset:0 sc1\n"
      "ds_write_b128 v178, v[120:123] offset:80\n"
      "ds_write_b128 v231, v[124:127] offset:96\n"
      "ds_write_b128 v178, v[128:131] offset:112\n"
      "v_max3_f32 v220, v68, v69, v70\n"
      "v_min3_f32 v208, v68, v69, v70\n"
      "v_max3_f32 v220, v220, v71, v72\n"
      "v_min3_f32 v208, v208, v71, v72\n"
      "v_max3_f32 v220, v220, v73, v74\n"
      "v_min3_f32 v208, v208, v73, v74\n"
      "v_max3_f32 v220, v220, v75, v76\n"
      "v_min3_f32 v208, v208, v75, v76\n"
      "v_max3_f32 v220, v220, v77, v78\n"
      "v_min3_f32 v208, v208, v77, v78\n"
      "v_max3_f32 v220, v220, v79, v80\n"
      "v_min3_f32 v208, v208, v79, v80\n"
      "v_max3_f32 v220, v220, v81, v82\n"
      "buffer_load_dwordx4 v199, s[12:15], s94 offen offset:1024 lds\n"
      "v_min3_f32 v208, v208, v81, v82\n"
      "v_max3_f32 v220, v220, v83, v84\n"
      "v_min3_f32 v208, v208, v83, v84\n"
      "v_max3_f32 v220, v220, v85, v86\n"
      "v_min3_f32 v208, v208, v85, v86\n"
      "v_max_f32 v220, v220, v87\n"
      "v_min_f32 v208, v208, v87\n"
      "v_mul_f32 v220, v220, v204\n"
      "v_mul_f32 v208, v208, v204\n"
      "v_pk_fma_f32 v[202:203], v[68:69], v[204:205], v[220:221] op_sel_hi:[1,0,0] neg_lo:[0,0,1] neg_hi:[0,0,1]\n"
      "v_pk_fma_f32 v[232:233], v[68:69], v[204:205], v[208:209] op_sel_hi:[1,0,0] neg_lo:[0,1,0] neg_hi:[0,1,0]\n"
      "v_pk_fma_f32 v[196:197], v[70:71], v[204:205], v[220:221] op_sel_hi:[1,0,0] neg_lo:[0,0,1] neg_hi:[0,0,1]\n"
      "v_pk_fma_f32 v[226:227], v[70:71], v[204:205], v[208:209] op_sel_hi:[1,0,0] neg_lo:[0,1,0] neg_hi:[0,1,0]\n"
      "v_pk_fma_f32 v[188:189], v[72:73], v[204:205], v[220:221] op_sel_hi:[1,0,0] neg_lo:[0,0,1] neg_hi:[0,0,1]\n"
      "v_pk_fma_f32 v[224:225], v[72:73], v[204:205], v[208:209] op_sel_hi:[1,0,0] neg_lo:[0,1,0] neg_hi:[0,1,0]\n"
      "v_pk_fma_f32 v[174:175], v[74:75], v[204:205], v[220:221] op_sel_hi:[1,0,0] neg_lo:[0,0,1] neg_hi:[0,0,1]\n"
      "v_pk_fma_f32 v[218:219], v[74:75], v[204:205], v[208:209] op_sel_hi:[1,0,0] neg_lo:[0,1,0] neg_hi:[0,1,0]\n"
      "v_pk_fma_f32 v[164:165], v[76:77], v[204:205], v[220:221] op_sel_hi:[1,0,0] neg_lo:[0,0,1] neg_hi:[0,0,1]\n"
      "v_pk_fma_f32 v[216:217], v[76:77], v[204:205], v[208:209] op_sel_hi:[1,0,0] neg_lo:[0,1,0] neg_hi:[0,1,0]\n"
      "v_pk_fma_f32 v[254:255], v[78:79], v[204:205], v[220:221] op_sel_hi:[1,0,0] neg_lo:[0,0,1] neg_hi:[0,0,1]\n"
      "v_pk_fma_f32 v[214:215], v[78:79], v[204:205], v[208:209] op_sel_hi:[1,0,0] neg_lo:[0,1,0] neg_hi:[0,1,0]\n"
      "buffer_load_dwordx4 v194, s[12:15], s94 offen offset:2048 lds\n"
      "v_pk_fma_f32 v[252:253], v[80:81], v[204:205], v[220:221] op_sel_hi:[1,0,0] neg_lo:[0,0,1] neg_hi:[0,0,1]\n"
      "v_pk_fma_f32 v[212:213], v[80:81], v[204:205], v[208:209] op_sel_hi:[1,0,0] neg_lo:[0,1,0] neg_hi:[0,1,0]\n"
      "v_pk_fma_f32 v[242:243], v[82:83], v[204:205], v[220:221] op_sel_hi:[1,0,0] neg_lo:[0,0,1] neg_hi:[0,0,1]\n"
      "v_pk_fma_f32 v[182:183], v[82:83], v[204:205], v[208:209] op_sel_hi:[1,0,0] neg_lo:[0,1,0] neg_hi:[0,1,0]\n"
      "v_pk_fma_f32 v[240:241], v[84:85], v[204:205], v[220:221] op_sel_hi:[1,0,0] neg_lo:[0,0,1] neg_hi:[0,0,1]\n"
      "v_pk_fma_f32 v[180:181], v[84:85], v[204:205], v[208:209] op_sel_hi:[1,0,0] neg_lo:[0,1,0] neg_hi:[0,1,0]\n"
      "v_pk_fma_f32 v[234:235], v[86:87], v[204:205], v[220:221] op_sel_hi:[1,0,0] neg_lo:[0,0,1] neg_hi:[0,0,1]\n"
      "v_pk_fma_f32 v[162:163], v[86:87], v[204:205], v[208:209] op_sel_hi:[1,0,0] neg_lo:[0,1,0] neg_hi:[0,1,0]\n"
      "v_exp_f32 v202, v202\n"
      "v_exp_f32 v232, v232\n"
      "v_exp_f32 v203, v203\n"
      "v_exp_f32 v233, v233\n"
      "v_exp_f32 v196, v196\n"
      "buffer_load_dwordx4 v191, s[12:15], s94 offen offset:3072 lds\n"
      "v_exp_f32 v226, v226\n"
      "v_exp_f32 v197, v197\n"
      "v_exp_f32 v227, v227\n"
      "v_exp_f32 v188, v188\n"
      "v_exp_f32 v224, v224\n"
      "v_exp_f32 v189, v189\n"
      "v_exp_f32 v225, v225\n"
      "v_exp_f32 v174, v174\n"
      "v_exp_f32 v218, v218\n"
      "v_exp_f32 v175, v175\n"
      "v_exp_f32 v219, v219\n"
      "s_add_u32 m0, s85, 65536\n"
      "s_nop 0\n"
      "buffer_load_dwordx4 v186, s[12:15], s94 offen offset:0 lds\n"
      "v_exp_f32 v164, v164\n"
      "v_exp_f32 v216, v216\n"
      "v_exp_f32 v165, v165\n"
      "v_exp_f32 v217, v217\n"
      "v_exp_f32 v254, v254\n"
      "v_exp_f32 v214, v214\n"
      "v_exp_f32 v255, v255\n"
      "v_exp_f32 v215, v215\n"
      "v_exp_f32 v252, v252\n"
      "v_exp_f32 v212, v212\n"
      "v_exp_f32 v253, v253\n"
      "s_add_u32 m0, s85, 0\n"
      "s_add_u32 s94, s78, 0\n"
      "buffer_load_dwordx4 v207, s[12:15], s94 offen offset:0 lds\n"
      "v_exp_f32 v213, v213\n"
      "v_exp_f32 v242, v242\n"
      "v_exp_f32 v182, v182\n"
      "v_exp_f32 v243, v243\n"
      "v_exp_f32 v183, v183\n"
      "v_exp_f32 v240, v240\n"
      "v_exp_f32 v180, v180\n"
      "v_exp_f32 v241, v241\n"
      "v_exp_f32 v181, v181\n"
      "v_exp_f32 v234, v234\n"
      "buffer_load_dwordx4 v199, s[12:15], s94 offen offset:1024 lds\n"
      "v_exp_f32 v162, v162\n"
      "v_exp_f32 v235, v235\n"
      "v_exp_f32 v163, v163\n"
      "v_pk_add_f32 v[220:221], v[202:203], v[196:197]\n"
      "v_pk_add_f32 v[208:209], v[232:233], v[226:227]\n"
      "v_pk_add_f32 v[220:221], v[220:221], v[188:189]\n"
      "v_pk_add_f32 v[208:209], v[208:209], v[224:225]\n"
      "v_pk_add_f32 v[220:221], v[220:221], v[174:175]\n"
      "v_pk_add_f32 v[208:209], v[208:209], v[218:219]\n"
      "v_pk_add_f32 v[220:221], v[220:221], v[164:165]\n"
      "v_pk_add_f32 v[208:209], v[208:209], v[216:217]\n"
      "v_pk_add_f32 v[220:221], v[220:221], v[254:255]\n"
      "v_pk_add_f32 v[208:209], v[208:209], v[214:215]\n"
      "v_pk_add_f32 v[220:221], v[220:221], v[252:253]\n"
      "v_pk_add_f32 v[208:209], v[208:209], v[212:213]\n"
      "buffer_load_dwordx4 v194, s[12:15], s94 offen offset:2048 lds\n"
      "v_pk_add_f32 v[220:221], v[220:221], v[242:243]\n"
      "v_pk_add_f32 v[208:209], v[208:209], v[182:183]\n"
      "v_pk_add_f32 v[220:221], v[220:221], v[240:241]\n"
      "v_pk_add_f32 v[208:209], v[208:209], v[180:181]\n"
      "v_pk_add_f32 v[220:221], v[220:221], v[234:235]\n"
      "v_pk_add_f32 v[208:209], v[208:209], v[162:163]\n"
      "v_add_f32 v220, v220, v221\n"
      "v_add_f32 v208, v208, v209\n"
      "v_rcp_f32 v220, v220\n"
      "v_rcp_f32 v208, v208\n"
      "v_pk_mul_f32 v[202:203], v[202:203], v[220:221] op_sel_hi:[1,0]\n"
      "v_pk_mul_f32 v[196:197], v[196:197], v[220:221] op_sel_hi:[1,0]\n"
      "v_pk_mul_f32 v[188:189], v[188:189], v[220:221] op_sel_hi:[1,0]\n"
      "v_pk_mul_f32 v[174:175], v[174:175], v[220:221] op_sel_hi:[1,0]\n"
      "v_pk_mul_f32 v[164:165], v[164:165], v[220:221] op_sel_hi:[1,0]\n"
      "v_pk_mul_f32 v[254:255], v[254:255], v[220:221] op_sel_hi:[1,0]\n"
      "buffer_load_dwordx4 v191, s[12:15], s94 offen offset:3072 lds\n"
      "v_pk_mul_f32 v[252:253], v[252:253], v[220:221] op_sel_hi:[1,0]\n"
      "v_pk_mul_f32 v[242:243], v[242:243], v[220:221] op_sel_hi:[1,0]\n"
      "v_pk_mul_f32 v[240:241], v[240:241], v[220:221] op_sel_hi:[1,0]\n"
      "v_pk_mul_f32 v[234:235], v[234:235], v[220:221] op_sel_hi:[1,0]\n"
      "v_cvt_pk_bf16_f32 v100, v202, v203\n"
      "v_cvt_pk_bf16_f32 v101, v196, v197\n"
      "v_cvt_pk_bf16_f32 v102, v188, v189\n"
      "v_cvt_pk_bf16_f32 v103, v174, v175\n"
      "v_cvt_pk_bf16_f32 v104, v164, v165\n"
      "v_cvt_pk_bf16_f32 v105, v254, v255\n"
      "v_cvt_pk_bf16_f32 v106, v252, v253\n"
      "v_cvt_pk_bf16_f32 v107, v242, v243\n"
      "v_cvt_pk_bf16_f32 v108, v240, v241\n"
      "v_cvt_pk_bf16_f32 v109, v234, v235\n"
      "v_pk_mul_f32 v[232:233], v[232:233], v[208:209] op_sel_hi:[1,0]\n"
      "v_pk_mul_f32 v[226:227], v[226:227], v[208:209] op_sel_hi:[1,0]\n"
      "v_pk_mul_f32 v[224:225], v[224:225], v[208:209] op_sel_hi:[1,0]\n"
      "v_pk_mul_f32 v[218:219], v[218:219], v[208:209] op_sel_hi:[1,0]\n"
      "v_pk_mul_f32 v[216:217], v[216:217], v[208:209] op_sel_hi:[1,0]\n"
      "v_pk_mul_f32 v[214:215], v[214:215], v[208:209] op_sel_hi:[1,0]\n"
      "v_pk_mul_f32 v[212:213], v[212:213], v[208:209] op_sel_hi:[1,0]\n"
      "s_add_u32 m0, s85, 4096\n"
      "s_nop 0\n"
      "buffer_load_dwordx4 v186, s[12:15], s94 offen offset:0 lds\n"
      "v_pk_mul_f32 v[182:183], v[182:183], v[208:209] op_sel_hi:[1,0]\n"
      "v_pk_mul_f32 v[180:181], v[180:181], v[208:209] op_sel_hi:[1,0]\n"
      "v_pk_mul_f32 v[162:163], v[162:163], v[208:209] op_sel_hi:[1,0]\n"
      "v_cvt_pk_bf16_f32 v110, v232, v233\n"
      "v_cvt_pk_bf16_f32 v111, v226, v227\n"
      "v_cvt_pk_bf16_f32 v112, v224, v225\n"
      "v_cvt_pk_bf16_f32 v113, v218, v219\n"
      "v_cvt_pk_bf16_f32 v114, v216, v217\n"
      "v_cvt_pk_bf16_f32 v115, v214, v215\n"
      "v_cvt_pk_bf16_f32 v116, v212, v213\n"
      "v_cvt_pk_bf16_f32 v117, v182, v183\n"
      "v_cvt_pk_bf16_f32 v118, v180, v181\n"
      "v_cvt_pk_bf16_f32 v119, v162, v163\n"
      "ds_write_b128 v231, v[100:103] offset:0\n"
      "ds_write_b128 v178, v[104:107] offset:16\n"
      "ds_write_b128 v231, v[108:111] offset:32\n"
      "ds_write_b128 v178, v[112:115] offset:48\n"
      "ds_write_b128 v231, v[116:119] offset:64\n"
      "v_accvgpr_read_b32 v249, a255\n"
      "v_accvgpr_read_b32 v246, a254\n"
      "v_accvgpr_read_b32 v244, a253\n"
      "v_accvgpr_read_b32 v238, a252\n"
      "v_accvgpr_read_b32 v231, a251\n"
      "v_accvgpr_read_b32 v222, a250\n"
      "v_accvgpr_read_b32 v211, a249\n"
      "s_waitcnt vmcnt(9)\n"
      "buffer_store_dwordx2 v[166:167], v251, s[36:39], 0 offen sc1\n"
      "s_waitcnt lgkmcnt(0)\n"
      "s_barrier\n"
      "ds_read_b64_tr_b16 a[12:13], v246 offset:0\n"
      "ds_read_b64_tr_b16 a[14:15], v246 offset:2304\n"
      "ds_read_b64_tr_b16 a[0:1], v249 offset:0\n"
      "ds_read_b64_tr_b16 a[2:3], v249 offset:2304\n"
      "ds_read_b64_tr_b16 a[4:5], v249 offset:32\n"
      "ds_read_b64_tr_b16 a[6:7], v249 offset:2336\n"
      "ds_read_b64_tr_b16 a[8:9], v249 offset:64\n"
      "ds_read_b64_tr_b16 a[10:11], v249 offset:2368\n"
      "ds_read_b64_tr_b16 a[56:57], v246 offset:9216\n"
      "ds_read_b64_tr_b16 a[58:59], v246 offset:11520\n"
      "ds_read_b64_tr_b16 a[44:45], v249 offset:9216\n"
      "ds_read_b64_tr_b16 a[46:47], v249 offset:11520\n"
      "ds_read_b64_tr_b16 a[48:49], v249 offset:9248\n"
      "ds_read_b64_tr_b16 a[50:51], v249 offset:11552\n"
      "ds_read_b64_tr_b16 a[52:53], v249 offset:9280\n"
      "ds_read_b64_tr_b16 a[54:55], v249 offset:11584\n"
      "s_waitcnt lgkmcnt(12)\n"
      "v_mfma_f32_16x16x32_bf16 v[4:7], a[0:3], a[12:15], 0\n"
      "s_waitcnt lgkmcnt(10)\n"
      "v_mfma_f32_16x16x32_bf16 v[8:11], a[4:7], a[12:15], 0\n"
      "s_waitcnt lgkmcnt(8)\n"
      "v_mfma_f32_16x16x32_bf16 v[12:15], a[8:11], a[12:15], 0\n"
      "ds_read_b64_tr_b16 a[72:73], v246 offset:18432\n"
      "ds_read_b64_tr_b16 a[74:75], v246 offset:20736\n"
      "ds_read_b64_tr_b16 a[60:61], v249 offset:18432\n"
      "ds_read_b64_tr_b16 a[62:63], v249 offset:20736\n"
      "ds_read_b64_tr_b16 a[64:65], v249 offset:18464\n"
      "ds_read_b64_tr_b16 a[66:67], v249 offset:20768\n"
      "ds_read_b64_tr_b16 a[68:69], v249 offset:18496\n"
      "ds_read_b64_tr_b16 a[70:71], v249 offset:20800\n"
      "s_waitcnt lgkmcnt(12)\n"
      "v_mfma_f32_16x16x32_bf16 v[4:7], a[44:47], a[56:59], v[4:7]\n"
      "s_waitcnt lgkmcnt(10)\n"
      "v_mfma_f32_16x16x32_bf16 v[8:11], a[48:51], a[56:59], v[8:11]\n"
      "s_waitcnt lgkmcnt(8)\n"
      "v_mfma_f32_16x16x32_bf16 v[12:15], a[52:55], a[56:59], v[12:15]\n"
      "ds_read_b64_tr_b16 a[88:89], v246 offset:27648\n"
      "ds_read_b64_tr_b16 a[90:91], v246 offset:29952\n"
      "ds_read_b64_tr_b16 a[76:77], v249 offset:27648\n"
      "ds_read_b64_tr_b16 a[78:79], v249 offset:29952\n"
      "ds_read_b64_tr_b16 a[80:81], v249 offset:27680\n"
      "ds_read_b64_tr_b16 a[82:83], v249 offset:29984\n"
      "ds_read_b64_tr_b16 a[84:85], v249 offset:27712\n"
      "ds_read_b64_tr_b16 a[86:87], v249 offset:30016\n"
      "s_waitcnt lgkmcnt(12)\n"
      "v_mfma_f32_16x16x32_bf16 v[4:7], a[60:63], a[72:75], v[4:7]\n"
      "s_waitcnt lgkmcnt(10)\n"
      "v_mfma_f32_16x16x32_bf16 v[8:11], a[64:67], a[72:75], v[8:11]\n"
      "s_waitcnt lgkmcnt(8)\n"
      "v_mfma_f32_16x16x32_bf16 v[12:15], a[68:71], a[72:75], v[12:15]\n"
      "s_waitcnt lgkmcnt(4)\n"
      "v_mfma_f32_16x16x32_bf16 v[4:7], a[76:79], a[88:91], v[4:7]\n"
      "s_waitcnt lgkmcnt(2)\n"
      "v_mfma_f32_16x16x32_bf16 v[8:11], a[80:83], a[88:91], v[8:11]\n"
      "s_waitcnt lgkmcnt(0)\n"
      "v_mfma_f32_16x16x32_bf16 v[12:15], a[84:87], a[88:91], v[12:15]\n"
      "s_nop 3\n"
      "v_cvt_pk_bf16_f32 v212, v4, v5\n"
      "v_cvt_pk_bf16_f32 v213, v6, v7\n"
      "v_cvt_pk_bf16_f32 v214, v8, v9\n"
      "v_cvt_pk_bf16_f32 v215, v10, v11\n"
      "v_cvt_pk_bf16_f32 v216, v12, v13\n"
      "v_cvt_pk_bf16_f32 v217, v14, v15\n"
      "ds_write_b128 v244, v[212:215]\n"
      "ds_write_b64 v238, v[216:217]\n"
      "s_waitcnt lgkmcnt(0)\n"
      "s_barrier\n"
      "ds_read_b128 v[4:7], v231\n"
      "s_waitcnt lgkmcnt(0)\n"
      "buffer_store_dwordx4 v[4:7], v222, s[36:39], 0 offen sc1\n"
      "s_add_u32 m0, s82, 86016\n"
      "s_add_u32 s94, s76, 0\n"
      "buffer_load_dwordx4 v185, s[20:23], s94 offen offset:0 lds\n"
      "buffer_load_dwordx4 v177, s[20:23], s94 offen offset:1024 lds\n"
      "v_cvt_pk_bf16_f32 v16, v36, v37\n"
      "buffer_load_dwordx4 v200, s[20:23], s94 offen offset:2048 lds\n"
      "v_cvt_pk_bf16_f32 v17, v38, v39\n"
      "v_cvt_pk_bf16_f32 v18, v40, v41\n"
      "v_cvt_pk_bf16_f32 v19, v42, v43\n"
      "v_cvt_pk_bf16_f32 v20, v44, v45\n"
      "v_cvt_pk_bf16_f32 v21, v46, v47\n"
      "v_cvt_pk_bf16_f32 v22, v48, v49\n"
      "v_cvt_pk_bf16_f32 v23, v50, v51\n"
      "v_cvt_pk_bf16_f32 v24, v60, v61\n"
      "buffer_load_dwordx4 v173, s[20:23], s94 offen offset:3072 lds\n"
      "v_cvt_pk_bf16_f32 v25, v62, v63\n"
      "v_mov_b32 v26, 0\n"
      "v_mov_b32 v27, 0\n"
      "v_mfma_f32_32x32x16_bf16 v[68:83], a[20:23], v[16:19], 0\n"
      "v_mfma_f32_32x32x16_bf16 v[84:99], a[32:35], v[16:19], 0\n"
      "s_add_u32 m0, s82, 90112\n"
      "s_nop 0\n"
      "buffer_load_dwordx4 v172, s[20:23], s94 offen offset:0 lds\n"
      "v_mfma_f32_32x32x16_bf16 v[68:83], a[24:27], v[20:23], v[68:83]\n"
      "v_mfma_f32_32x32x16_bf16 v[84:99], a[36:39], v[20:23], v[84:99]\n"
      "buffer_load_dwordx4 v169, s[20:23], s94 offen offset:1024 lds\n"
      "v_mfma_f32_32x32x16_bf16 v[68:83], a[28:31], v[24:27], v[68:83]\n"
      "v_mfma_f32_32x32x16_bf16 v[84:99], a[40:43], v[24:27], v[84:99]\n"
      "buffer_load_dwordx4 v168, s[20:23], s94 offen offset:2048 lds\n"
      "v_mov_b32 v251, 0x3f80\n"
      "v_mov_b32 v246, 0x3f800000\n"
      "v_bfe_u32 v238, v1, 4, 1\n"
      "v_lshl_add_u32 v244, v238, 3, 1\n"
      "v_and_b32 v231, 15, v1\n"
      "v_cmp_eq_u32 vcc, v244, v231\n"
      "s_nop 1\n"
      "v_cndmask_b32 v249, 0, v246, vcc\n"
      "v_lshlrev_b32 v222, 3, v238\n"
      "v_cmp_eq_u32 vcc, v222, v231\n"
      "s_nop 1\n"
      "v_cndmask_b32 v180, v249, v251, vcc\n"
      "v_add_u32 v201, 3, v222\n"
      "v_cmp_eq_u32 vcc, v201, v231\n"
      "s_nop 1\n"
      "v_cndmask_b32 v206, 0, v246, vcc\n"
      "s_add_u32 m0, s85, 20480\n"
      "s_add_u32 s94, s78, 163840\n"
      "buffer_load_dwordx4 v207, s[12:15], s94 offen offset:0 lds\n"
      "v_add_u32 v198, 2, v222\n"
      "v_cmp_eq_u32 vcc, v198, v231\n"
      "s_nop 1\n"
      "v_cndmask_b32 v181, v206, v251, vcc\n"
      "v_add_u32 v190, 5, v222\n"
      "v_cmp_eq_u32 vcc, v190, v231\n"
      "s_nop 1\n"
      "v_cndmask_b32 v195, 0, v246, vcc\n"
      "v_add_u32 v187, 4, v222\n"
      "v_cmp_eq_u32 vcc, v187, v231\n"
      "s_nop 1\n"
      "v_cndmask_b32 v182, v195, v251, vcc\n"
      "v_add_u32 v176, 7, v222\n"
      "v_cmp_eq_u32 vcc, v176, v231\n"
      "s_nop 1\n"
      "v_cndmask_b32 v184, 0, v246, vcc\n"
      "v_add_u32 v171, 6, v222\n"
      "v_cmp_eq_u32 vcc, v171, v231\n"
      "s_nop 1\n"
      "v_cndmask_b32 v183, v184, v251, vcc\n"
      "s_mul_i32 s90, s86, 0xe000\n"
      "s_add_u32 s98, s90, 0x71000\n"
      "s_lshl_b32 s87, s101, 5\n"
      "v_and_b32 v221, 31, v1\n"
      "buffer_load_dwordx4 v199, s[12:15], s94 offen offset:1024 lds\n"
      "v_add_u32 v236, s87, v221\n"
      "v_lshrrev_b32 v209, 5, v1\n"
      "v_mul_u32_u24 v220, 0xe00, v209\n"
      "v_lshl_add_u32 v237, v236, 4, v220\n"
      "v_add_u32 v2, s98, v237\n"
      "s_add_u32 s79, s90, 0x71800\n"
      "v_add_u32 v203, s79, v237\n"
      "v_mov_b32 v202, 0x40000000\n"
      "v_cmp_gt_u32 vcc, 0x52, v236\n"
      "s_nop 1\n"
      "v_cndmask_b32 v208, v202, v203, vcc\n"
      "s_lshl_b32 s81, s101, 9\n"
      "s_add_u32 s66, s81, 0x22000\n"
      "v_lshrrev_b32 v179, 4, v1\n"
      "v_lshrrev_b32 v188, 1, v179\n"
      "v_lshl_add_u32 v189, v188, 4, v231\n"
      "buffer_load_dwordx4 v194, s[12:15], s94 offen offset:2048 lds\n"
      "v_lshl_add_u32 v196, v189, 4, v222\n"
      "v_add_u32 v197, s66, v196\n"
      "v_bfe_u32 v254, v1, 3, 2\n"
      "v_and_b32 v253, 3, v1\n"
      "v_lshl_add_u32 v255, v254, 2, v253\n"
      "v_add_u32 v164, 4, v255\n"
      "v_add_u32 v243, 16, v253\n"
      "v_add_u32 v242, -4, v255\n"
      "v_cmp_eq_u32 vcc, 0, v254\n"
      "s_nop 1\n"
      "v_cndmask_b32 v252, v242, v243, vcc\n"
      "v_bfe_u32 v241, v1, 2, 1\n"
      "v_cmp_eq_u32 vcc, 1, v241\n"
      "s_nop 1\n"
      "v_cndmask_b32 v165, v252, v164, vcc\n"
      "v_mul_u32_u24 v240, 0x690, v241\n"
      "v_lshl_add_u32 v174, v165, 4, v240\n"
      "v_add_u32 v175, 0x22000, v174\n"
      "buffer_load_dwordx4 v191, s[12:15], s94 offen offset:3072 lds\n"
      "v_mov_b32 v235, 0x27000\n"
      "v_cmp_eq_u32 vcc, v241, v209\n"
      "s_nop 1\n"
      "v_cndmask_b32 v178, v235, v175, vcc\n"
      "v_add_u32 v225, 12, v253\n"
      "v_mul_u32_u24 v224, 12, v241\n"
      "v_sub_u32 v226, v225, v224\n"
      "v_cmp_eq_u32 vcc, 0, v254\n"
      "s_nop 1\n"
      "v_cndmask_b32 v227, 20, v226, vcc\n"
      "v_lshl_add_u32 v232, v227, 4, v240\n"
      "v_add_u32 v233, 0x22000, v232\n"
      "v_lshrrev_b32 v218, 1, v254\n"
      "v_lshl_add_u32 v219, v218, 1, v241\n"
      "v_cmp_eq_u32 vcc, v219, v209\n"
      "s_nop 1\n"
      "v_cndmask_b32 v234, v235, v233, vcc\n"
      "s_mul_i32 s80, s86, 5\n"
      "v_mad_u32_u24 v215, v209, 3, s80\n"
      "s_add_u32 m0, s85, 24576\n"
      "s_nop 0\n"
      "buffer_load_dwordx4 v186, s[12:15], s94 offen offset:0 lds\n"
      "v_and_b32 v216, 3, v215\n"
      "v_lshrrev_b32 v213, 2, v215\n"
      "s_lshl_b32 s74, s101, 1\n"
      "s_lshl3_add_u32 s75, s88, s74\n"
      "v_add_u32 v161, s75, v238\n"
      "v_mul_u32_u24 v162, 0x5000, v161\n"
      "v_lshl_add_u32 v163, v231, 4, v162\n"
      "v_add_u32 v212, 0x151000, v163\n"
      "v_lshl_add_u32 v214, v213, 10, v212\n"
      "v_lshl_add_u32 v217, v216, 8, v214\n"
      "s_add_u32 s65, s80, 1\n"
      "v_mad_u32_u24 v158, v209, 3, s65\n"
      "v_and_b32 v159, 3, v158\n"
      "v_lshrrev_b32 v156, 2, v158\n"
      "v_lshl_add_u32 v157, v156, 10, v212\n"
      "v_lshl_add_u32 v160, v159, 8, v157\n"
      "s_add_u32 s71, s80, 2\n"
      "s_lshr_b32 s34, s71, 2\n"
      "s_lshl_b32 s35, s34, 10\n"
      "s_and_b32 s69, s71, 3\n"
      "s_lshl_b32 s70, s69, 8\n"
      "s_add_u32 s64, s35, s70\n"
      "v_lshl_add_u32 v154, v209, 30, v212\n"
      "s_add_u32 m0, s85, 81920\n"
      "s_add_u32 s94, s78, 655360\n"
      "buffer_load_dwordx4 v207, s[12:15], s94 offen offset:0 lds\n"
      "v_add_u32 v155, s64, v154\n"
      "s_lshl_b32 s62, s86, 11\n"
      "s_lshl_b32 s61, s88, 7\n"
      "s_add_u32 s63, s62, s61\n"
      "s_add_u32 s68, s63, 0x1000\n"
      "v_lshl_add_u32 v153, v1, 3, s68\n"
      "s_lshl2_add_u32 s57, s88, s101\n"
      "s_lshl_b32 s58, s57, 9\n"
      "s_lshl3_add_u32 s59, s86, s58\n"
      "s_add_u32 s60, s59, 0x9000\n"
      "v_lshl_add_u32 v151, v1, 7, s60\n"
      "v_cmp_gt_u32 vcc, 4, v1\n"
      "s_nop 1\n"
      "v_cndmask_b32 v152, v202, v151, vcc\n"
      "buffer_load_dwordx4 v199, s[12:15], s94 offen offset:1024 lds\n"
      "s_mov_b32 s98, 7168\n"
      "s_mov_b32 s90, 14336\n"
      "s_mov_b32 s87, 21504\n"
      "s_mov_b32 s79, 28672\n"
      "s_mov_b32 s66, 35840\n"
      "s_mov_b32 s81, 43008\n"
      "s_mov_b32 s80, 50176\n"
      "s_waitcnt vmcnt(14)\n"
      "s_barrier\n"
      "buffer_store_dwordx2 v[166:167], v211, s[36:39], 0 offen sc1\n"
      "s_mov_b64 exec, s[92:93]\n"
      "buffer_load_dwordx2 v[236:237], v228, s[36:39], 0 offen sc1\n"
      "s_mov_b64 exec, -1\n"
      "v_max3_f32 v232, v68, v69, v70\n"
      "v_min3_f32 v220, v68, v69, v70\n"
      "v_max3_f32 v232, v232, v71, v72\n"
      "v_min3_f32 v220, v220, v71, v72\n"
      "v_max3_f32 v232, v232, v73, v74\n"
      "v_min3_f32 v220, v220, v73, v74\n"
      "v_max3_f32 v232, v232, v75, v76\n"
      "v_min3_f32 v220, v220, v75, v76\n"
      "v_max3_f32 v232, v232, v77, v78\n"
      "v_min3_f32 v220, v220, v77, v78\n"
      "v_max3_f32 v232, v232, v79, v80\n"
      "buffer_load_dwordx4 v194, s[12:15], s94 offen offset:2048 lds\n"
      "v_min3_f32 v220, v220, v79, v80\n"
      "v_max3_f32 v232, v232, v81, v82\n"
      "v_min3_f32 v220, v220, v81, v82\n"
      "v_max3_f32 v232, v232, v83, v84\n"
      "v_min3_f32 v220, v220, v83, v84\n"
      "v_max3_f32 v232, v232, v85, v86\n"
      "v_min3_f32 v220, v220, v85, v86\n"
      "v_max_f32 v232, v232, v87\n"
      "v_min_f32 v220, v220, v87\n"
      "v_mul_f32 v232, v232, v204\n"
      "v_mul_f32 v220, v220, v204\n"
      "v_pk_fma_f32 v[218:219], v[68:69], v[204:205], v[232:233] op_sel_hi:[1,0,0] neg_lo:[0,0,1] neg_hi:[0,0,1]\n"
      "v_pk_fma_f32 v[226:227], v[68:69], v[204:205], v[220:221] op_sel_hi:[1,0,0] neg_lo:[0,1,0] neg_hi:[0,1,0]\n"
      "v_pk_fma_f32 v[202:203], v[70:71], v[204:205], v[232:233] op_sel_hi:[1,0,0] neg_lo:[0,0,1] neg_hi:[0,0,1]\n"
      "buffer_load_dwordx4 v191, s[12:15], s94 offen offset:3072 lds\n"
      "v_pk_fma_f32 v[224:225], v[70:71], v[204:205], v[220:221] op_sel_hi:[1,0,0] neg_lo:[0,1,0] neg_hi:[0,1,0]\n"
      "v_pk_fma_f32 v[188:189], v[72:73], v[204:205], v[232:233] op_sel_hi:[1,0,0] neg_lo:[0,0,1] neg_hi:[0,0,1]\n"
      "v_pk_fma_f32 v[214:215], v[72:73], v[204:205], v[220:221] op_sel_hi:[1,0,0] neg_lo:[0,1,0] neg_hi:[0,1,0]\n"
      "v_pk_fma_f32 v[174:175], v[74:75], v[204:205], v[232:233] op_sel_hi:[1,0,0] neg_lo:[0,0,1] neg_hi:[0,0,1]\n"
      "v_pk_fma_f32 v[212:213], v[74:75], v[204:205], v[220:221] op_sel_hi:[1,0,0] neg_lo:[0,1,0] neg_hi:[0,1,0]\n"
      "v_pk_fma_f32 v[164:165], v[76:77], v[204:205], v[232:233] op_sel_hi:[1,0,0] neg_lo:[0,0,1] neg_hi:[0,0,1]\n"
      "v_pk_fma_f32 v[158:159], v[76:77], v[204:205], v[220:221] op_sel_hi:[1,0,0] neg_lo:[0,1,0] neg_hi:[0,1,0]\n"
      "v_pk_fma_f32 v[162:163], v[78:79], v[204:205], v[232:233] op_sel_hi:[1,0,0] neg_lo:[0,0,1] neg_hi:[0,0,1]\n"
      "s_add_u32 m0, s85, 86016\n"
      "s_nop 0\n"
      "buffer_load_dwordx4 v186, s[12:15], s94 offen offset:0 lds\n"
      "v_pk_fma_f32 v[156:157], v[78:79], v[204:205], v[220:221] op_sel_hi:[1,0,0] neg_lo:[0,1,0] neg_hi:[0,1,0]\n"
      "v_pk_fma_f32 v[254:255], v[80:81], v[204:205], v[232:233] op_sel_hi:[1,0,0] neg_lo:[0,0,1] neg_hi:[0,0,1]\n"
      "v_pk_fma_f32 v[150:151], v[80:81], v[204:205], v[220:221] op_sel_hi:[1,0,0] neg_lo:[0,1,0] neg_hi:[0,1,0]\n"
      "v_pk_fma_f32 v[252:253], v[82:83], v[204:205], v[232:233] op_sel_hi:[1,0,0] neg_lo:[0,0,1] neg_hi:[0,0,1]\n"
      "v_pk_fma_f32 v[148:149], v[82:83], v[204:205], v[220:221] op_sel_hi:[1,0,0] neg_lo:[0,1,0] neg_hi:[0,1,0]\n"
      "v_pk_fma_f32 v[242:243], v[84:85], v[204:205], v[232:233] op_sel_hi:[1,0,0] neg_lo:[0,0,1] neg_hi:[0,0,1]\n"
      "v_pk_fma_f32 v[146:147], v[84:85], v[204:205], v[220:221] op_sel_hi:[1,0,0] neg_lo:[0,1,0] neg_hi:[0,1,0]\n"
      "v_pk_fma_f32 v[240:241], v[86:87], v[204:205], v[232:233] op_sel_hi:[1,0,0] neg_lo:[0,0,1] neg_hi:[0,0,1]\n"
      "v_pk_fma_f32 v[144:145], v[86:87], v[204:205], v[220:221] op_sel_hi:[1,0,0] neg_lo:[0,1,0] neg_hi:[0,1,0]\n"
      "s_cmp_eq_u32 s101, 2\n"
      "s_cselect_b32 s75, 1792, 0\n"
      "v_lshrrev_b32 v249, 5, v1\n"
      "v_mad_u32_u24 v251, v249, s75, v230\n"
      "v_subrev_u32 v249, s75, v251\n"
      "s_waitcnt vmcnt(12)\n"
      "ds_read_b64_tr_b16 a[0:1], v251 offset:0\n"
      "ds_read_b64_tr_b16 a[2:3], v249 offset:3584\n"
      "ds_read_b64_tr_b16 a[4:5], v251 offset:32\n"
      "ds_read_b64_tr_b16 a[6:7], v249 offset:3616\n"
      "ds_read_b64_tr_b16 a[8:9], v251 offset:64\n"
      "ds_read_b64_tr_b16 a[10:11], v249 offset:3648\n"
      "ds_read_b64_tr_b16 a[12:13], v251 offset:96\n"
      "ds_read_b64_tr_b16 a[14:15], v249 offset:3680\n"
      "ds_read_b64_tr_b16 a[16:17], v251 offset:128\n"
      "ds_read_b64_tr_b16 a[18:19], v249 offset:3712\n"
      "ds_read_b64_tr_b16 a[20:21], v251 offset:160\n"
      "ds_read_b64_tr_b16 a[22:23], v249 offset:3744\n"
      "ds_read_b64_tr_b16 a[24:25], v251 offset:192\n"
      "ds_read_b64_tr_b16 a[26:27], v249 offset:3776\n"
      "v_exp_f32 v218, v218\n"
      "v_exp_f32 v226, v226\n"
      "v_exp_f32 v219, v219\n"
      "v_exp_f32 v227, v227\n"
      "v_exp_f32 v202, v202\n"
      "v_exp_f32 v224, v224\n"
      "s_waitcnt lgkmcnt(0)\n"
      "s_mov_b64 exec, s[92:93]\n"
      "s_waitcnt vmcnt(3)\n"
      "v_cmp_ne_u64 vcc, v[236:237], v[166:167]\n"
      "s_nop 0\n"
      "s_cbranch_vccz L1_%=\n"
      "s_mov_b32 s75, 200000\n"
      "L2_%=:\n"
      "s_sleep 1\n"
      "buffer_load_dwordx2 v[142:143], v228, s[36:39], 0 offen sc1\n"
      "s_waitcnt vmcnt(0)\n"
      "v_cmp_ne_u64 vcc, v[142:143], v[166:167]\n"
      "s_sub_u32 s75, s75, 1\n"
      "s_cbranch_scc1 L3_%=\n"
      "s_cbranch_vccnz L2_%=\n"
      "L3_%=:\n"
      "L1_%=:\n"
      "s_mov_b64 exec, -1\n"
      "v_exp_f32 v203, v203\n"
      "v_exp_f32 v225, v225\n"
      "v_exp_f32 v188, v188\n"
      "v_exp_f32 v214, v214\n"
      "v_exp_f32 v189, v189\n"
      "s_add_u32 m0, s82, 86016\n"
      "s_add_u32 s94, s76, 442368\n"
      "buffer_load_dwordx4 v185, s[20:23], s94 offen offset:0 lds\n"
      "v_exp_f32 v215, v215\n"
      "v_exp_f32 v174, v174\n"
      "v_exp_f32 v212, v212\n"
      "v_exp_f32 v175, v175\n"
      "v_exp_f32 v213, v213\n"
      "v_exp_f32 v164, v164\n"
      "buffer_load_dwordx4 v177, s[20:23], s94 offen offset:1024 lds\n"
      "v_exp_f32 v158, v158\n"
      "v_exp_f32 v165, v165\n"
      "v_exp_f32 v159, v159\n"
      "v_exp_f32 v162, v162\n"
      "v_exp_f32 v156, v156\n"
      "buffer_load_dwordx4 v200, s[20:23], s94 offen offset:2048 lds\n"
      "v_exp_f32 v163, v163\n"
      "v_exp_f32 v157, v157\n"
      "s_mov_b64 exec, 0xffff\n"
      "buffer_load_dwordx2 v[236:237], v153, s[36:39], 0 offen sc1\n"
      "s_mov_b64 exec, -1\n"
      "v_exp_f32 v254, v254\n"
      "v_exp_f32 v150, v150\n"
      "v_exp_f32 v255, v255\n"
      "buffer_load_dwordx4 v173, s[20:23], s94 offen offset:3072 lds\n"
      "v_exp_f32 v151, v151\n"
      "v_exp_f32 v252, v252\n"
      "v_exp_f32 v148, v148\n"
      "v_exp_f32 v253, v253\n"
      "v_exp_f32 v149, v149\n"
      "v_exp_f32 v242, v242\n"
      "s_add_u32 m0, s82, 90112\n"
      "s_nop 0\n"
      "buffer_load_dwordx4 v172, s[20:23], s94 offen offset:0 lds\n"
      "v_exp_f32 v146, v146\n"
      "v_exp_f32 v243, v243\n"
      "v_exp_f32 v147, v147\n"
      "v_exp_f32 v240, v240\n"
      "v_exp_f32 v144, v144\n"
      "buffer_load_dwordx4 v169, s[20:23], s94 offen offset:1024 lds\n"
      "v_exp_f32 v241, v241\n"
      "v_exp_f32 v145, v145\n"
      "v_pk_add_f32 v[232:233], v[218:219], v[202:203]\n"
      "v_pk_add_f32 v[220:221], v[226:227], v[224:225]\n"
      "v_pk_add_f32 v[232:233], v[232:233], v[188:189]\n"
      "v_pk_add_f32 v[220:221], v[220:221], v[214:215]\n"
      "v_pk_add_f32 v[232:233], v[232:233], v[174:175]\n"
      "buffer_load_dwordx4 v168, s[20:23], s94 offen offset:2048 lds\n"
      "v_pk_add_f32 v[220:221], v[220:221], v[212:213]\n"
      "v_pk_add_f32 v[232:233], v[232:233], v[164:165]\n"
      "v_pk_add_f32 v[220:221], v[220:221], v[158:159]\n"
      "v_pk_add_f32 v[232:233], v[232:233], v[162:163]\n"
      "v_pk_add_f32 v[220:221], v[220:221], v[156:157]\n"
      "v_pk_add_f32 v[232:233], v[232:233], v[254:255]\n"
      "v_pk_add_f32 v[220:221], v[220:221], v[150:151]\n"
      "v_pk_add_f32 v[232:233], v[232:233], v[252:253]\n"
      "s_add_u32 s96, s97, 0\n"
      "buffer_load_dwordx4 a[148:151], v239, s[36:39], s96 offen offset:0 sc1\n"
      "v_pk_add_f32 v[220:221], v[220:221], v[148:149]\n"
      "v_pk_add_f32 v[232:233], v[232:233], v[242:243]\n"
      "v_pk_add_f32 v[220:221], v[220:221], v[146:147]\n"
      "v_pk_add_f32 v[232:233], v[232:233], v[240:241]\n"
      "v_pk_add_f32 v[220:221], v[220:221], v[144:145]\n"
      "v_add_f32 v232, v232, v233\n"
      "v_add_f32 v220, v220, v221\n"
      "v_rcp_f32 v232, v232\n"
      "buffer_load_dwordx4 a[152:155], v239, s[36:39], s96 offen offset:1024 sc1\n"
      "v_rcp_f32 v220, v220\n"
      "s_mov_b64 exec, 0xffff\n"
      "s_waitcnt vmcnt(6)\n"
      "v_cmp_ne_u64 vcc, v[236:237], v[166:167]\n"
      "s_nop 0\n"
      "s_cbranch_vccz L5_%=\n"
      "s_mov_b32 s93, 200000\n"
      "L6_%=:\n"
      "s_sleep 1\n"
      "buffer_load_dwordx2 v[142:143], v153, s[36:39], 0 offen sc1\n"
      "s_waitcnt vmcnt(0)\n"
      "v_cmp_ne_u64 vcc, v[142:143], v[166:167]\n"
      "s_sub_u32 s93, s93, 1\n"
      "s_cbranch_scc1 L7_%=\n"
      "s_cbranch_vccnz L6_%=\n"
      "L7_%=:\n"
      "L5_%=:\n"
      "s_mov_b64 exec, -1\n"
      "buffer_load_dwordx4 a[84:87], v2, s[36:39], 0 offen sc1\n"
      "buffer_load_dwordx4 a[88:91], v208, s[36:39], 0 offen sc1\n"
      "buffer_load_dwordx4 a[92:95], v2, s[36:39], s98 offen sc1\n"
      "buffer_load_dwordx4 a[96:99], v208, s[36:39], s98 offen sc1\n"
      "buffer_load_dwordx4 a[100:103], v2, s[36:39], s90 offen sc1\n"
      "buffer_load_dwordx4 a[104:107], v208, s[36:39], s90 offen sc1\n"
      "buffer_load_dwordx4 a[108:111], v2, s[36:39], s87 offen sc1\n"
      "buffer_load_dwordx4 a[112:115], v208, s[36:39], s87 offen sc1\n"
      "buffer_load_dwordx4 a[116:119], v2, s[36:39], s79 offen sc1\n"
      "buffer_load_dwordx4 a[120:123], v208, s[36:39], s79 offen sc1\n"
      "buffer_load_dwordx4 a[124:127], v2, s[36:39], s66 offen sc1\n"
      "buffer_load_dwordx4 a[128:131], v208, s[36:39], s66 offen sc1\n"
      "buffer_load_dwordx4 a[132:135], v2, s[36:39], s81 offen sc1\n"
      "buffer_load_dwordx4 a[136:139], v208, s[36:39], s81 offen sc1\n"
      "buffer_load_dwordx4 a[140:143], v2, s[36:39], s80 offen sc1\n"
      "buffer_load_dwordx4 a[144:147], v208, s[36:39], s80 offen sc1\n"
      "v_pk_mul_f32 v[218:219], v[218:219], v[232:233] op_sel_hi:[1,0]\n"
      "v_pk_mul_f32 v[202:203], v[202:203], v[232:233] op_sel_hi:[1,0]\n"
      "v_pk_mul_f32 v[188:189], v[188:189], v[232:233] op_sel_hi:[1,0]\n"
      "v_pk_mul_f32 v[174:175], v[174:175], v[232:233] op_sel_hi:[1,0]\n"
      "v_pk_mul_f32 v[164:165], v[164:165], v[232:233] op_sel_hi:[1,0]\n"
      "v_pk_mul_f32 v[162:163], v[162:163], v[232:233] op_sel_hi:[1,0]\n"
      "v_pk_mul_f32 v[254:255], v[254:255], v[232:233] op_sel_hi:[1,0]\n"
      "s_mov_b32 s96, 4096\n"
      "buffer_load_dwordx4 a[156:159], v239, s[36:39], s96 offen offset:0 sc1\n"
      "v_pk_mul_f32 v[252:253], v[252:253], v[232:233] op_sel_hi:[1,0]\n"
      "v_pk_mul_f32 v[242:243], v[242:243], v[232:233] op_sel_hi:[1,0]\n"
      "v_pk_mul_f32 v[240:241], v[240:241], v[232:233] op_sel_hi:[1,0]\n"
      "v_cvt_pk_bf16_f32 v100, v218, v219\n"
      "v_cvt_pk_bf16_f32 v101, v202, v203\n"
      "v_cvt_pk_bf16_f32 v104, v188, v189\n"
      "v_cvt_pk_bf16_f32 v105, v174, v175\n"
      "v_cvt_pk_bf16_f32 v108, v164, v165\n"
      "v_cvt_pk_bf16_f32 v109, v162, v163\n"
      "v_cvt_pk_bf16_f32 v112, v254, v255\n"
      "v_cvt_pk_bf16_f32 v113, v252, v253\n"
      "v_cvt_pk_bf16_f32 v102, v242, v243\n"
      "s_add_u32 s96, s83, 0\n"
      "buffer_load_dwordx4 a[160:163], v239, s[36:39], s96 offen offset:0 sc1\n"
      "v_cvt_pk_bf16_f32 v103, v240, v241\n"
      "v_pk_mul_f32 v[226:227], v[226:227], v[220:221] op_sel_hi:[1,0]\n"
      "v_pk_mul_f32 v[224:225], v[224:225], v[220:221] op_sel_hi:[1,0]\n"
      "v_pk_mul_f32 v[214:215], v[214:215], v[220:221] op_sel_hi:[1,0]\n"
      "v_pk_mul_f32 v[212:213], v[212:213], v[220:221] op_sel_hi:[1,0]\n"
      "v_pk_mul_f32 v[158:159], v[158:159], v[220:221] op_sel_hi:[1,0]\n"
      "v_pk_mul_f32 v[156:157], v[156:157], v[220:221] op_sel_hi:[1,0]\n"
      "v_pk_mul_f32 v[150:151], v[150:151], v[220:221] op_sel_hi:[1,0]\n"
      "v_pk_mul_f32 v[148:149], v[148:149], v[220:221] op_sel_hi:[1,0]\n"
      "buffer_load_dwordx4 a[164:167], v239, s[36:39], s96 offen offset:1024 sc1\n"
      "v_pk_mul_f32 v[146:147], v[146:147], v[220:221] op_sel_hi:[1,0]\n"
      "v_pk_mul_f32 v[144:145], v[144:145], v[220:221] op_sel_hi:[1,0]\n"
      "v_cvt_pk_bf16_f32 v106, v226, v227\n"
      "v_cvt_pk_bf16_f32 v107, v224, v225\n"
      "v_cvt_pk_bf16_f32 v110, v214, v215\n"
      "v_cvt_pk_bf16_f32 v111, v212, v213\n"
      "v_cvt_pk_bf16_f32 v114, v158, v159\n"
      "v_cvt_pk_bf16_f32 v115, v156, v157\n"
      "v_cvt_pk_bf16_f32 v116, v150, v151\n"
      "v_cvt_pk_bf16_f32 v117, v148, v149\n"
      "v_cvt_pk_bf16_f32 v118, v146, v147\n"
      "v_cvt_pk_bf16_f32 v119, v144, v145\n"
      "s_cmp_eq_u32 s101, 3\n"
      "s_cselect_b32 s98, 1792, 0\n"
      "v_lshrrev_b32 v244, 5, v1\n"
      "v_mad_u32_u24 v249, v244, s98, v230\n"
      "v_subrev_u32 v244, s98, v249\n"
      "s_waitcnt vmcnt(21)\n"
      "ds_read_b64_tr_b16 a[28:29], v249 offset:0\n"
      "ds_read_b64_tr_b16 a[30:31], v244 offset:3584\n"
      "ds_read_b64_tr_b16 a[32:33], v249 offset:32\n"
      "ds_read_b64_tr_b16 a[34:35], v244 offset:3616\n"
      "ds_read_b64_tr_b16 a[36:37], v249 offset:64\n"
      "ds_read_b64_tr_b16 a[38:39], v244 offset:3648\n"
      "ds_read_b64_tr_b16 a[40:41], v249 offset:96\n"
      "ds_read_b64_tr_b16 a[42:43], v244 offset:3680\n"
      "ds_read_b64_tr_b16 a[44:45], v249 offset:128\n"
      "ds_read_b64_tr_b16 a[46:47], v244 offset:3712\n"
      "ds_read_b64_tr_b16 a[48:49], v249 offset:160\n"
      "ds_read_b64_tr_b16 a[50:51], v244 offset:3744\n"
      "ds_read_b64_tr_b16 a[52:53], v249 offset:192\n"
      "ds_read_b64_tr_b16 a[54:55], v244 offset:3776\n"
      "s_waitcnt vmcnt(18)\n"
      "v_mfma_f32_16x16x32_bf16 v[220:223], v[180:183], a[84:87], 0\n"
      "s_waitcnt vmcnt(17)\n"
      "v_mfma_f32_16x16x32_bf16 v[224:227], v[180:183], a[88:91], 0\n"
      "s_waitcnt vmcnt(16)\n"
      "v_mfma_f32_16x16x32_bf16 v[220:223], v[180:183], a[92:95], v[220:223]\n"
      "s_waitcnt vmcnt(15)\n"
      "v_mfma_f32_16x16x32_bf16 v[224:227], v[180:183], a[96:99], v[224:227]\n"
      "s_waitcnt vmcnt(14)\n"
      "v_mfma_f32_16x16x32_bf16 v[220:223], v[180:183], a[100:103], v[220:223]\n"
      "s_waitcnt vmcnt(13)\n"
      "v_mfma_f32_16x16x32_bf16 v[224:227], v[180:183], a[104:107], v[224:227]\n"
      "s_waitcnt vmcnt(12)\n"
      "v_mfma_f32_16x16x32_bf16 v[220:223], v[180:183], a[108:111], v[220:223]\n"
      "s_waitcnt vmcnt(11)\n"
      "v_mfma_f32_16x16x32_bf16 v[224:227], v[180:183], a[112:115], v[224:227]\n"
      "s_waitcnt vmcnt(10)\n"
      "v_mfma_f32_16x16x32_bf16 v[220:223], v[180:183], a[116:119], v[220:223]\n"
      "s_waitcnt vmcnt(9)\n"
      "v_mfma_f32_16x16x32_bf16 v[224:227], v[180:183], a[120:123], v[224:227]\n"
      "s_waitcnt vmcnt(8)\n"
      "v_mfma_f32_16x16x32_bf16 v[220:223], v[180:183], a[124:127], v[220:223]\n"
      "s_waitcnt vmcnt(7)\n"
      "v_mfma_f32_16x16x32_bf16 v[224:227], v[180:183], a[128:131], v[224:227]\n"
      "s_waitcnt vmcnt(6)\n"
      "v_mfma_f32_16x16x32_bf16 v[220:223], v[180:183], a[132:135], v[220:223]\n"
      "s_waitcnt vmcnt(5)\n"
      "v_mfma_f32_16x16x32_bf16 v[224:227], v[180:183], a[136:139], v[224:227]\n"
      "s_waitcnt vmcnt(4)\n"
      "v_mfma_f32_16x16x32_bf16 v[220:223], v[180:183], a[140:143], v[220:223]\n"
      "s_waitcnt vmcnt(3)\n"
      "v_mfma_f32_16x16x32_bf16 v[224:227], v[180:183], a[144:147], v[224:227]\n"
      "s_waitcnt lgkmcnt(0)\n"
      "s_add_u32 m0, s82, 86016\n"
      "s_add_u32 s94, s76, 884736\n"
      "buffer_load_dwordx4 v185, s[20:23], s94 offen offset:0 lds\n"
      "buffer_load_dwordx4 v177, s[20:23], s94 offen offset:1024 lds\n"
      "buffer_load_dwordx4 v200, s[20:23], s94 offen offset:2048 lds\n"
      "buffer_load_dwordx4 v173, s[20:23], s94 offen offset:3072 lds\n"
      "s_add_u32 m0, s82, 90112\n"
      "s_nop 0\n"
      "buffer_load_dwordx4 v172, s[20:23], s94 offen offset:0 lds\n"
      "buffer_load_dwordx4 v169, s[20:23], s94 offen offset:1024 lds\n"
      "buffer_load_dwordx4 v168, s[20:23], s94 offen offset:2048 lds\n"
      "v_cvt_pk_bf16_f32 v156, v220, v221\n"
      "v_cvt_pk_bf16_f32 v157, v222, v223\n"
      "ds_write_b64 v197, v[156:157] offset:0\n"
      "v_cvt_pk_bf16_f32 v212, v224, v225\n"
      "v_cvt_pk_bf16_f32 v213, v226, v227\n"
      "ds_write_b64 v197, v[212:213] offset:2048\n"
      "s_waitcnt lgkmcnt(0)\n"
      "s_barrier\n"
      "ds_read_b128 v[212:215], v234 offset:0\n"
      "ds_read_b128 v[156:159], v178 offset:0\n"
      "ds_read_b128 v[252:255], v234 offset:336\n"
      "ds_read_b128 v[240:243], v178 offset:336\n"
      "ds_read_b128 v[124:127], v234 offset:672\n"
      "ds_read_b128 v[120:123], v178 offset:672\n"
      "ds_read_b128 v[132:135], v234 offset:1008\n"
      "ds_read_b128 v[128:131], v178 offset:1008\n"
      "ds_read_b128 v[140:143], v234 offset:1344\n"
      "ds_read_b128 v[136:139], v178 offset:1344\n"
      "s_waitcnt lgkmcnt(9)\n"
      "v_mfma_f32_32x32x16_bf16 v[84:99], v[212:215], v[100:103], 0\n"
      "s_waitcnt lgkmcnt(7)\n"
      "v_mfma_f32_32x32x16_bf16 v[84:99], v[252:255], v[104:107], v[84:99]\n"
      "s_waitcnt lgkmcnt(5)\n"
      "v_mfma_f32_32x32x16_bf16 v[84:99], v[124:127], v[108:111], v[84:99]\n"
      "s_waitcnt lgkmcnt(3)\n"
      "v_mfma_f32_32x32x16_bf16 v[84:99], v[132:135], v[112:115], v[84:99]\n"
      "s_waitcnt lgkmcnt(1)\n"
      "v_mfma_f32_32x32x16_bf16 v[84:99], v[140:143], v[116:119], v[84:99]\n"
      "v_mfma_f32_32x32x16_bf16 v[68:83], v[156:159], v[100:103], 0\n"
      "v_mfma_f32_32x32x16_bf16 v[68:83], v[240:243], v[104:107], v[68:83]\n"
      "v_mfma_f32_32x32x16_bf16 v[68:83], v[120:123], v[108:111], v[68:83]\n"
      "v_mfma_f32_32x32x16_bf16 v[68:83], v[128:131], v[112:115], v[68:83]\n"
      "s_waitcnt lgkmcnt(0)\n"
      "v_mfma_f32_32x32x16_bf16 v[68:83], v[136:139], v[116:119], v[68:83]\n"
      "s_nop 5\n"
      "v_add_f32 v250, 0x358637bd, v88\n"
      "v_rcp_f32 v250, v250\n"
      "s_nop 0\n"
      "v_pk_mul_f32 v[84:85], v[84:85], v[250:251] op_sel_hi:[1,0]\n"
      "v_pk_mul_f32 v[86:87], v[86:87], v[250:251] op_sel_hi:[1,0]\n"
      "s_nop 0\n"
      "v_pk_mul_f32 v[68:69], v[68:69], v[250:251] op_sel_hi:[1,0]\n"
      "v_pk_mul_f32 v[70:71], v[70:71], v[250:251] op_sel_hi:[1,0]\n"
      "v_pk_mul_f32 v[72:73], v[72:73], v[250:251] op_sel_hi:[1,0]\n"
      "v_pk_mul_f32 v[74:75], v[74:75], v[250:251] op_sel_hi:[1,0]\n"
      "v_pk_mul_f32 v[76:77], v[76:77], v[250:251] op_sel_hi:[1,0]\n"
      "v_pk_mul_f32 v[78:79], v[78:79], v[250:251] op_sel_hi:[1,0]\n"
      "v_pk_mul_f32 v[80:81], v[80:81], v[250:251] op_sel_hi:[1,0]\n"
      "v_pk_mul_f32 v[82:83], v[82:83], v[250:251] op_sel_hi:[1,0]\n"
      "v_cvt_pk_bf16_f32 v240, v84, v85\n"
      "v_cvt_pk_bf16_f32 v241, v86, v87\n"
      "v_cvt_pk_bf16_f32 v156, v68, v69\n"
      "v_cvt_pk_bf16_f32 v157, v70, v71\n"
      "v_permlane32_swap_b32 v240, v242\n"
      "v_cvt_pk_bf16_f32 v158, v72, v73\n"
      "v_permlane32_swap_b32 v241, v243\n"
      "v_cvt_pk_bf16_f32 v159, v74, v75\n"
      "buffer_store_dwordx4 v[156:159], v217, s[36:39], 0 offen sc1\n"
      "v_cvt_pk_bf16_f32 v212, v76, v77\n"
      "buffer_store_dwordx4 v[240:243], v155, s[36:39], 0 offen sc1\n"
      "v_cvt_pk_bf16_f32 v213, v78, v79\n"
      "v_cvt_pk_bf16_f32 v214, v80, v81\n"
      "v_cvt_pk_bf16_f32 v215, v82, v83\n"
      "buffer_store_dwordx4 v[212:215], v160, s[36:39], 0 offen sc1\n"
      "s_waitcnt vmcnt(3)\n"
      "ds_read_b64_tr_b16 a[56:57], v230 offset:0\n"
      "ds_read_b64_tr_b16 a[58:59], v230 offset:3584\n"
      "ds_read_b64_tr_b16 a[60:61], v230 offset:32\n"
      "ds_read_b64_tr_b16 a[62:63], v230 offset:3616\n"
      "ds_read_b64_tr_b16 a[64:65], v230 offset:64\n"
      "ds_read_b64_tr_b16 a[66:67], v230 offset:3648\n"
      "ds_read_b64_tr_b16 a[68:69], v230 offset:96\n"
      "ds_read_b64_tr_b16 a[70:71], v230 offset:3680\n"
      "ds_read_b64_tr_b16 a[72:73], v230 offset:128\n"
      "ds_read_b64_tr_b16 a[74:75], v230 offset:3712\n"
      "ds_read_b64_tr_b16 a[76:77], v230 offset:160\n"
      "ds_read_b64_tr_b16 a[78:79], v230 offset:3744\n"
      "ds_read_b64_tr_b16 a[80:81], v230 offset:192\n"
      "ds_read_b64_tr_b16 a[82:83], v230 offset:3776\n"
      "s_mul_i32 s87, s101, 0xc00\n"
      "s_add_u32 s90, s87, 0x23000\n"
      "s_and_b32 s66, s89, 1\n"
      "s_lshl_b32 s79, s66, 6\n"
      "s_add_u32 s98, s90, s79\n"
      "v_lshrrev_b32 v238, 4, v1\n"
      "v_and_b32 v211, 15, v1\n"
      "v_mul_u32_u24 v231, 0xc0, v211\n"
      "v_lshl_add_u32 v244, v238, 3, v231\n"
      "v_add_u32 v249, s98, v244\n"
      "s_mul_i32 s81, s66, 0x60\n"
      "s_sub_u32 s92, 0x80, s81\n"
      "s_add_u32 s93, s90, s92\n"
      "v_add_u32 v206, s93, v244\n"
      "ds_read_b64 v[44:45], v249 offset:0\n"
      "ds_read_b64 v[46:47], v249 offset:32\n"
      "ds_read_b64 v[48:49], v249 offset:64\n"
      "ds_read_b64 v[50:51], v249 offset:96\n"
      "ds_read_b64 v[52:53], v206 offset:0\n"
      "s_waitcnt lgkmcnt(5)\n"
      "s_add_u32 m0, s82, 86016\n"
      "s_add_u32 s94, s76, 1327104\n"
      "buffer_load_dwordx4 v185, s[20:23], s94 offen offset:0 lds\n"
      "buffer_load_dwordx4 v177, s[20:23], s94 offen offset:1024 lds\n"
      "buffer_load_dwordx4 v200, s[20:23], s94 offen offset:2048 lds\n"
      "buffer_load_dwordx4 v173, s[20:23], s94 offen offset:3072 lds\n"
      "s_add_u32 m0, s82, 90112\n"
      "s_nop 0\n"
      "buffer_load_dwordx4 v172, s[20:23], s94 offen offset:0 lds\n"
      "buffer_load_dwordx4 v169, s[20:23], s94 offen offset:1024 lds\n"
      "buffer_load_dwordx4 v168, s[20:23], s94 offen offset:2048 lds\n"
      "s_lshr_b32 s66, s101, 1\n"
      "s_lshl1_add_u32 s79, s91, s66\n"
      "s_lshl_b32 s87, s79, 9\n"
      "s_lshr_b32 s92, s89, 1\n"
      "s_lshl_b32 s93, s92, 7\n"
      "s_add_u32 s90, s87, s93\n"
      "s_add_u32 s98, s90, 0x9000\n"
      "v_lshl_add_u32 v244, v1, 3, s98\n"
      "s_lshl2_add_u32 s75, s91, s101\n"
      "s_mul_i32 s80, s75, 0x5000\n"
      "s_add_u32 s81, s80, 0x151000\n"
      "v_add_u32 v238, s81, v3\n"
      "v_mov_b32 v211, 0\n"
      "v_mov_b32 v201, 0x40000000\n"
      "s_add_u32 s65, s101, s100\n"
      "s_lshl_b32 s74, s65, 6\n"
      "v_add_u32 v198, s74, v1\n"
      "v_cmp_eq_u32 vcc, 0, v198\n"
      "s_nop 1\n"
      "v_cndmask_b32 v231, v201, v211, vcc\n"
      "s_add_u32 s35, s80, 0x3d1000\n"
      "s_mul_i32 s69, s89, 5\n"
      "s_and_b32 s68, s89, 1\n"
      "s_add_u32 s70, s69, s68\n"
      "s_lshr_b32 s71, s70, 1\n"
      "s_lshl_b32 s34, s71, 10\n"
      "s_add_u32 s64, s35, s34\n"
      "v_add_u32 v195, s64, v3\n"
      "s_add_u32 s59, s69, 4\n"
      "s_lshl_b32 s58, s68, 2\n"
      "s_sub_u32 s60, s59, s58\n"
      "s_lshr_b32 s61, s60, 1\n"
      "s_lshl_b32 s62, s61, 10\n"
      "s_lshl_b32 s57, s68, 9\n"
      "s_add_u32 s63, s62, s57\n"
      "v_lshrrev_b32 v184, 5, v1\n"
      "v_add_u32 v176, s35, v3\n"
      "v_lshl_add_u32 v187, v184, 30, v176\n"
      "v_add_u32 v190, s63, v187\n"
      "s_lshl_b32 s54, s75, 10\n"
      "s_lshl_b32 s53, s89, 7\n"
      "s_add_u32 s55, s54, s53\n"
      "s_add_u32 s56, s55, 0x51000\n"
      "v_lshl_add_u32 v2, v1, 3, s56\n"
      "v_cmp_gt_u32 vcc, 16, v1\n"
      "s_nop 1\n"
      "v_cndmask_b32 v153, v201, v2, vcc\n"
      "s_lshr_b32 s48, s75, 1\n"
      "s_lshl_b32 s49, s48, 9\n"
      "s_and_b32 s46, s75, 1\n"
      "s_lshl_b32 s47, s46, 6\n"
      "s_add_u32 s50, s49, s47\n"
      "s_lshl3_add_u32 s51, s89, s50\n"
      "s_add_u32 s52, s51, 0x11000\n"
      "v_lshl_add_u32 v250, v1, 7, s52\n"
      "v_cmp_gt_u32 vcc, 4, v1\n"
      "s_nop 1\n"
      "v_cndmask_b32 v251, v201, v250, vcc\n"
      "v_subrev_u32 v247, s85, v248\n"
      "v_add_u32 v246, 0xffff1000, v247\n"
      "s_waitcnt lgkmcnt(4)\n"
      "v_lshlrev_b32 v56, 16, v44\n"
      "v_and_b32 v57, 0xffff0000, v44\n"
      "v_lshlrev_b32 v58, 16, v45\n"
      "v_and_b32 v59, 0xffff0000, v45\n"
      "s_waitcnt lgkmcnt(3)\n"
      "v_lshlrev_b32 v60, 16, v46\n"
      "v_and_b32 v61, 0xffff0000, v46\n"
      "v_lshlrev_b32 v62, 16, v47\n"
      "v_and_b32 v63, 0xffff0000, v47\n"
      "s_waitcnt lgkmcnt(2)\n"
      "v_lshlrev_b32 v64, 16, v48\n"
      "v_and_b32 v65, 0xffff0000, v48\n"
      "v_lshlrev_b32 v66, 16, v49\n"
      "v_and_b32 v67, 0xffff0000, v49\n"
      "s_waitcnt lgkmcnt(1)\n"
      "v_lshlrev_b32 v68, 16, v50\n"
      "v_and_b32 v69, 0xffff0000, v50\n"
      "v_lshlrev_b32 v70, 16, v51\n"
      "v_and_b32 v71, 0xffff0000, v51\n"
      "s_waitcnt lgkmcnt(0)\n"
      "v_lshlrev_b32 v72, 16, v52\n"
      "v_and_b32 v73, 0xffff0000, v52\n"
      "v_lshlrev_b32 v74, 16, v53\n"
      "v_and_b32 v75, 0xffff0000, v53\n"
      "s_mov_b32 s98, 4096\n"
      "s_mov_b32 s90, 8192\n"
      "s_mov_b32 s87, 12288\n"
      "s_mov_b32 s85, 16384\n"
      "s_waitcnt vmcnt(7)\n"
      "buffer_store_dwordx2 v[166:167], v152, s[36:39], 0 offen sc1\n"
      "s_waitcnt lgkmcnt(0)\n"
      "s_barrier\n"
      "s_add_u32 s96, s97, 5120\n"
      "buffer_load_dwordx4 a[168:171], v239, s[36:39], s96 offen offset:0 sc1\n"
      "buffer_load_dwordx4 a[172:175], v239, s[36:39], s96 offen offset:1024 sc1\n"
      "s_mov_b32 s96, 9216\n"
      "buffer_load_dwordx4 a[176:179], v239, s[36:39], s96 offen offset:0 sc1\n"
      "s_add_u32 s96, s83, 5120\n"
      "buffer_load_dwordx4 a[180:183], v239, s[36:39], s96 offen offset:0 sc1\n"
      "buffer_load_dwordx4 a[184:187], v239, s[36:39], s96 offen offset:1024 sc1\n"
      "s_add_u32 s96, s97, 10240\n"
      "buffer_load_dwordx4 a[188:191], v239, s[36:39], s96 offen offset:0 sc1\n"
      "v_mov_b32 v196, 0x3f803f80\n"
      "v_mov_b32 v197, 0x3f803f80\n"
      "v_mov_b32 v198, 0x3f803f80\n"
      "v_mov_b32 v199, 0x3f803f80\n"
      "s_cmp_eq_u32 s101, 0\n"
      "s_cselect_b32 s77, 1792, 0\n"
      "v_lshrrev_b32 v245, 5, v1\n"
      "v_mad_u32_u24 v250, v245, s77, v230\n"
      "v_subrev_u32 v245, s77, v250\n"
      "s_waitcnt vmcnt(7)\n"
      "ds_read_b64_tr_b16 v[180:181], v250 offset:0\n"
      "ds_read_b64_tr_b16 v[182:183], v245 offset:3584\n"
      "ds_read_b64_tr_b16 v[240:241], v250 offset:32\n"
      "ds_read_b64_tr_b16 v[242:243], v245 offset:3616\n"
      "ds_read_b64_tr_b16 v[252:253], v250 offset:64\n"
      "ds_read_b64_tr_b16 v[254:255], v245 offset:3648\n"
      "ds_read_b64_tr_b16 v[232:233], v250 offset:96\n"
      "ds_read_b64_tr_b16 v[234:235], v245 offset:3680\n"
      "ds_read_b64_tr_b16 v[156:157], v250 offset:128\n"
      "ds_read_b64_tr_b16 v[158:159], v245 offset:3712\n"
      "ds_read_b64_tr_b16 v[160:161], v250 offset:160\n"
      "ds_read_b64_tr_b16 v[162:163], v245 offset:3744\n"
      "ds_read_b64_tr_b16 v[212:213], v250 offset:192\n"
      "ds_read_b64_tr_b16 v[214:215], v245 offset:3776\n"
      "s_waitcnt lgkmcnt(12)\n"
      "ds_write_b128 v193, v[180:183] offset:0\n"
      "v_mfma_f32_16x16x32_bf16 v[204:207], v[196:199], v[180:183], 0\n"
      "s_waitcnt lgkmcnt(11)\n"
      "ds_write_b128 v193, v[240:243] offset:1024\n"
      "v_mfma_f32_16x16x32_bf16 v[208:211], v[196:199], v[240:243], 0\n"
      "s_waitcnt lgkmcnt(10)\n"
      "ds_write_b128 v193, v[252:255] offset:2048\n"
      "v_mfma_f32_16x16x32_bf16 v[44:47], v[196:199], v[252:255], 0\n"
      "s_waitcnt lgkmcnt(9)\n"
      "ds_write_b128 v193, v[232:235] offset:3072\n"
      "v_mfma_f32_16x16x32_bf16 v[48:51], v[196:199], v[232:235], 0\n"
      "s_waitcnt lgkmcnt(8)\n"
      "ds_write_b128 v193, v[156:159] offset:4096\n"
      "v_mfma_f32_16x16x32_bf16 v[52:55], v[196:199], v[156:159], 0\n"
      "s_waitcnt lgkmcnt(7)\n"
      "ds_write_b128 v193, v[160:163] offset:5120\n"
      "v_mfma_f32_16x16x32_bf16 v[76:79], v[196:199], v[160:163], 0\n"
      "s_waitcnt lgkmcnt(6)\n"
      "ds_write_b128 v193, v[212:215] offset:6144\n"
      "v_mfma_f32_16x16x32_bf16 v[80:83], v[196:199], v[212:215], 0\n"
      "ds_read_b64_tr_b16 v[216:217], v246 offset:0\n"
      "ds_read_b64_tr_b16 v[218:219], v246 offset:2560\n"
      "ds_read_b64_tr_b16 v[220:221], v246 offset:32\n"
      "ds_read_b64_tr_b16 v[222:223], v246 offset:2592\n"
      "ds_read_b64_tr_b16 v[224:225], v246 offset:64\n"
      "ds_read_b64_tr_b16 v[226:227], v246 offset:2624\n"
      "ds_read_b64_tr_b16 v[4:5], v246 offset:96\n"
      "ds_read_b64_tr_b16 v[6:7], v246 offset:2656\n"
      "ds_read_b64_tr_b16 v[8:9], v246 offset:128\n"
      "ds_read_b64_tr_b16 v[10:11], v246 offset:2688\n"
      "ds_read_b64_tr_b16 v[12:13], v246 offset:5120\n"
      "ds_read_b64_tr_b16 v[14:15], v246 offset:7680\n"
      "ds_read_b64_tr_b16 v[16:17], v246 offset:5152\n"
      "ds_read_b64_tr_b16 v[18:19], v246 offset:7712\n"
      "ds_read_b64_tr_b16 v[84:85], v246 offset:5184\n"
      "ds_read_b64_tr_b16 v[86:87], v246 offset:7744\n"
      "ds_read_b64_tr_b16 v[180:181], v246 offset:5216\n"
      "ds_read_b64_tr_b16 v[182:183], v246 offset:7776\n"
      "ds_read_b64_tr_b16 v[240:241], v246 offset:5248\n"
      "ds_read_b64_tr_b16 v[242:243], v246 offset:7808\n"
      "ds_read_b64_tr_b16 v[252:253], v246 offset:10240\n"
      "ds_read_b64_tr_b16 v[254:255], v246 offset:12800\n"
      "ds_read_b64_tr_b16 v[232:233], v246 offset:10272\n"
      "ds_read_b64_tr_b16 v[234:235], v246 offset:12832\n"
      "ds_read_b64_tr_b16 v[156:157], v246 offset:10304\n"
      "ds_read_b64_tr_b16 v[158:159], v246 offset:12864\n"
      "ds_read_b64_tr_b16 v[160:161], v246 offset:10336\n"
      "ds_read_b64_tr_b16 v[162:163], v246 offset:12896\n"
      "ds_read_b64_tr_b16 v[212:213], v246 offset:10368\n"
      "ds_read_b64_tr_b16 v[214:215], v246 offset:12928\n"
      "s_mov_b32 s77, 200000\n"
      "s_mov_b64 exec, 0xffff\n"
      "s_branch L11_%=\n"
      "L9_%=:\n"
      "s_sleep 1\n"
      "L11_%=:\n"
      "buffer_load_dwordx2 v[248:249], v244, s[36:39], 0 offen sc1\n"
      "s_waitcnt vmcnt(0)\n"
      "v_cmp_ne_u64 vcc, v[248:249], v[166:167]\n"
      "s_sub_u32 s77, s77, 1\n"
      "s_cbranch_scc1 L10_%=\n"
      "s_cbranch_vccnz L9_%=\n"
      "L10_%=:\n"
      "s_mov_b64 exec, -1\n"
      "buffer_load_dwordx4 v[88:91], v238, s[36:39], 0 offen offset:0 sc1\n"
      "buffer_load_dwordx4 v[92:95], v238, s[36:39], 0 offen offset:1024 sc1\n"
      "buffer_load_dwordx4 v[96:99], v238, s[36:39], 0 offen offset:2048 sc1\n"
      "buffer_load_dwordx4 v[100:103], v238, s[36:39], 0 offen offset:3072 sc1\n"
      "buffer_load_dwordx4 v[104:107], v238, s[36:39], s98 offen offset:0 sc1\n"
      "buffer_load_dwordx4 v[108:111], v238, s[36:39], s98 offen offset:1024 sc1\n"
      "buffer_load_dwordx4 v[112:115], v238, s[36:39], s98 offen offset:2048 sc1\n"
      "buffer_load_dwordx4 v[116:119], v238, s[36:39], s98 offen offset:3072 sc1\n"
      "v_mfma_f32_16x16x32_bf16 v[204:207], v[196:199], a[0:3], v[204:207]\n"
      "s_lshl_b32 s81, s101, 1\n"
      "s_lshl3_add_u32 s92, s99, s81\n"
      "s_lshl_b32 s93, s92, 8\n"
      "v_mfma_f32_16x16x32_bf16 v[208:211], v[196:199], a[4:7], v[208:211]\n"
      "s_lshr_b32 s79, s95, 2\n"
      "s_lshl_b32 s80, s79, 7\n"
      "s_add_u32 s66, s93, s80\n"
      "v_mfma_f32_16x16x32_bf16 v[44:47], v[196:199], a[8:11], v[44:47]\n"
      "s_add_u32 s77, s66, 0x11000\n"
      "v_lshl_add_u32 v250, v1, 3, s77\n"
      "s_lshl_b32 s75, s92, 10\n"
      "v_mfma_f32_16x16x32_bf16 v[48:51], v[196:199], a[12:15], v[48:51]\n"
      "s_add_u32 s78, s75, 0x51000\n"
      "v_lshrrev_b32 v194, 4, v1\n"
      "v_and_b32 v191, 15, v1\n"
      "v_mfma_f32_16x16x32_bf16 v[52:55], v[196:199], a[16:19], v[52:55]\n"
      "v_lshlrev_b32 v192, 3, v191\n"
      "v_lshl_add_u32 v201, v194, 8, v192\n"
      "v_add_u32 v245, s78, v201\n"
      "v_mfma_f32_16x16x32_bf16 v[76:79], v[196:199], a[20:23], v[76:79]\n"
      "s_mul_i32 s65, s92, 0x5000\n"
      "s_add_u32 s74, s65, 0x3d1000\n"
      "v_add_u32 v184, s74, v3\n"
      "v_mfma_f32_16x16x32_bf16 v[80:83], v[196:199], a[24:27], v[80:83]\n"
      "s_add_u32 s34, s92, 1\n"
      "s_mul_i32 s35, s34, 0x5000\n"
      "s_add_u32 s64, s35, 0x3d1000\n"
      "v_mfma_f32_16x16x32_bf16 v[204:207], v[196:199], a[28:31], v[204:207]\n"
      "v_add_u32 v176, s64, v3\n"
      "s_mul_i32 s69, s92, 0xd800\n"
      "s_mul_i32 s60, s95, 7\n"
      "v_mfma_f32_16x16x32_bf16 v[208:211], v[196:199], a[32:35], v[208:211]\n"
      "s_and_b32 s59, s95, 1\n"
      "s_add_u32 s61, s60, s59\n"
      "s_mul_i32 s57, s95, 6\n"
      "v_mfma_f32_16x16x32_bf16 v[44:47], v[196:199], a[36:39], v[44:47]\n"
      "s_add_u32 s58, s57, 12\n"
      "s_cmp_lt_u32 s95, 12\n"
      "s_cselect_b32 s62, s61, s58\n"
      "s_lshr_b32 s63, s62, 1\n"
      "v_mfma_f32_16x16x32_bf16 v[48:51], v[196:199], a[40:43], v[48:51]\n"
      "s_lshl_b32 s68, s63, 10\n"
      "s_add_u32 s70, s69, s68\n"
      "s_add_u32 s71, s70, 0x651000\n"
      "v_mfma_f32_16x16x32_bf16 v[52:55], v[196:199], a[44:47], v[52:55]\n"
      "v_add_u32 v152, s71, v3\n"
      "s_mul_i32 s54, s34, 0xd800\n"
      "s_add_u32 s55, s54, s68\n"
      "v_mfma_f32_16x16x32_bf16 v[76:79], v[196:199], a[48:51], v[76:79]\n"
      "s_add_u32 s56, s55, 0x651000\n"
      "v_add_u32 v2, s56, v3\n"
      "s_add_u32 s46, s60, 6\n"
      "v_mfma_f32_16x16x32_bf16 v[80:83], v[196:199], a[52:55], v[80:83]\n"
      "s_mul_i32 s45, s59, 6\n"
      "s_sub_u32 s47, s46, s45\n"
      "s_lshr_b32 s48, s47, 1\n"
      "v_mfma_f32_16x16x32_bf16 v[204:207], v[196:199], a[56:59], v[204:207]\n"
      "s_lshl_b32 s49, s48, 10\n"
      "s_add_u32 s50, s69, s49\n"
      "s_and_b32 s43, s47, 1\n"
      "v_mfma_f32_16x16x32_bf16 v[208:211], v[196:199], a[60:63], v[208:211]\n"
      "s_lshl_b32 s44, s43, 9\n"
      "s_add_u32 s51, s50, s44\n"
      "s_add_u32 s52, s51, 0x651000\n"
      "v_mfma_f32_16x16x32_bf16 v[44:47], v[196:199], a[64:67], v[44:47]\n"
      "s_cmp_lt_u32 s95, 12\n"
      "s_cselect_b32 s53, s52, 0x40000000\n"
      "v_lshrrev_b32 v237, 5, v1\n"
      "v_lshl_add_u32 v248, v237, 30, v3\n"
      "v_mfma_f32_16x16x32_bf16 v[48:51], v[196:199], a[68:71], v[48:51]\n"
      "v_add_u32 v249, s53, v248\n"
      "s_add_u32 s27, s54, s49\n"
      "s_add_u32 s40, s27, s44\n"
      "v_mfma_f32_16x16x32_bf16 v[52:55], v[196:199], a[72:75], v[52:55]\n"
      "s_add_u32 s41, s40, 0x651000\n"
      "s_cmp_lt_u32 s95, 12\n"
      "s_cselect_b32 s42, s41, 0x40000000\n"
      "v_add_u32 v236, s42, v248\n"
      "v_mfma_f32_16x16x32_bf16 v[76:79], v[196:199], a[76:79], v[76:79]\n"
      "s_and_b32 s93, s101, 1\n"
      "s_and_b32 s81, s95, 3\n"
      "s_lshr_b32 s75, s101, 1\n"
      "v_mfma_f32_16x16x32_bf16 v[80:83], v[196:199], a[80:83], v[80:83]\n"
      "s_lshl1_add_u32 s78, s99, s75\n"
      "s_lshl_b32 s79, s78, 10\n"
      "s_lshr_b32 s65, s95, 2\n"
      "s_lshl_b32 s74, s65, 8\n"
      "s_add_u32 s80, s79, s74\n"
      "s_lshl4_add_u32 s92, s81, s80\n"
      "s_lshl3_add_u32 s66, s93, s92\n"
      "s_add_u32 s77, s66, 0x19000\n"
      "v_lshl_add_u32 v228, v1, 7, s77\n"
      "v_mov_b32 v203, 0x40000000\n"
      "v_cmp_gt_u32 vcc, 2, v1\n"
      "s_nop 1\n"
      "v_cndmask_b32 v229, v203, v228, vcc\n"
      "s_add_u32 s64, s66, 0x21000\n"
      "v_lshl_add_u32 v189, v1, 7, s64\n"
      "v_cmp_gt_u32 vcc, 2, v1\n"
      "s_nop 1\n"
      "v_cndmask_b32 v202, v203, v189, vcc\n"
      "s_lshl_b32 s35, s101, 2\n"
      "v_lshl_add_u32 v188, v191, 4, s35\n"
      "v_lshlrev_b32 v187, 4, v191\n"
      "s_lshl_b32 s69, s91, 10\n"
      "s_lshl_b32 s68, s101, 8\n"
      "s_add_u32 s70, s69, s68\n"
      "s_lshr_b32 s62, s89, 2\n"
      "s_lshl_b32 s63, s62, 7\n"
      "s_add_u32 s71, s70, s63\n"
      "s_add_u32 s34, s71, 0x19000\n"
      "v_lshl_add_u32 v186, v1, 3, s34\n"
      "s_add_u32 s61, s71, 0x21000\n"
      "v_lshl_add_u32 v179, v1, 3, s61\n"
      "v_add_u32 v178, 0x1e000, v3\n"
      "v_add_u32 v175, 0xfffeb000, v193\n"
      "v_add_u32 v174, 0xf000, v3\n"
      "s_waitcnt vmcnt(7) lgkmcnt(15)\n"
      "v_mfma_f32_16x16x32_bf16 v[56:59], v[216:219], v[88:91], v[56:59]\n"
      "ds_read_b64_tr_b16 v[216:217], v246 offset:15360\n"
      "ds_read_b64_tr_b16 v[218:219], v246 offset:17920\n"
      "buffer_load_dwordx4 v[120:123], v238, s[36:39], s90 offen offset:0 sc1\n"
      "v_mfma_f32_16x16x32_bf16 v[60:63], v[220:223], v[88:91], v[60:63]\n"
      "ds_read_b64_tr_b16 v[220:221], v246 offset:15392\n"
      "ds_read_b64_tr_b16 v[222:223], v246 offset:17952\n"
      "v_mfma_f32_16x16x32_bf16 v[64:67], v[224:227], v[88:91], v[64:67]\n"
      "ds_read_b64_tr_b16 v[224:225], v246 offset:15424\n"
      "ds_read_b64_tr_b16 v[226:227], v246 offset:17984\n"
      "v_mfma_f32_16x16x32_bf16 v[68:71], v[4:7], v[88:91], v[68:71]\n"
      "ds_read_b64_tr_b16 v[4:5], v246 offset:15456\n"
      "ds_read_b64_tr_b16 v[6:7], v246 offset:18016\n"
      "buffer_load_dwordx4 v[124:127], v238, s[36:39], s90 offen offset:1024 sc1\n"
      "v_mfma_f32_16x16x32_bf16 v[72:75], v[8:11], v[88:91], v[72:75]\n"
      "ds_read_b64_tr_b16 v[8:9], v246 offset:15488\n"
      "ds_read_b64_tr_b16 v[10:11], v246 offset:18048\n"
      "s_waitcnt vmcnt(8) lgkmcnt(15)\n"
      "v_mfma_f32_16x16x32_bf16 v[56:59], v[12:15], v[92:95], v[56:59]\n"
      "ds_read_b64_tr_b16 v[12:13], v246 offset:20480\n"
      "ds_read_b64_tr_b16 v[14:15], v246 offset:23040\n"
      "buffer_load_dwordx4 v[128:131], v238, s[36:39], s90 offen offset:2048 sc1\n"
      "v_mfma_f32_16x16x32_bf16 v[60:63], v[16:19], v[92:95], v[60:63]\n"
      "ds_read_b64_tr_b16 v[16:17], v246 offset:20512\n"
      "ds_read_b64_tr_b16 v[18:19], v246 offset:23072\n"
      "v_mfma_f32_16x16x32_bf16 v[64:67], v[84:87], v[92:95], v[64:67]\n"
      "ds_read_b64_tr_b16 v[84:85], v246 offset:20544\n"
      "ds_read_b64_tr_b16 v[86:87], v246 offset:23104\n"
      "v_mfma_f32_16x16x32_bf16 v[68:71], v[180:183], v[92:95], v[68:71]\n"
      "ds_read_b64_tr_b16 v[180:181], v246 offset:20576\n"
      "ds_read_b64_tr_b16 v[182:183], v246 offset:23136\n"
      "buffer_load_dwordx4 v[88:91], v238, s[36:39], s90 offen offset:3072 sc1\n"
      "v_mfma_f32_16x16x32_bf16 v[72:75], v[240:243], v[92:95], v[72:75]\n"
      "ds_read_b64_tr_b16 v[240:241], v246 offset:20608\n"
      "ds_read_b64_tr_b16 v[242:243], v246 offset:23168\n"
      "s_waitcnt vmcnt(9) lgkmcnt(15)\n"
      "v_mfma_f32_16x16x32_bf16 v[56:59], v[252:255], v[96:99], v[56:59]\n"
      "ds_read_b64_tr_b16 v[252:253], v246 offset:25600\n"
      "ds_read_b64_tr_b16 v[254:255], v246 offset:28160\n"
      "buffer_load_dwordx4 v[92:95], v238, s[36:39], s87 offen offset:0 sc1\n"
      "v_mfma_f32_16x16x32_bf16 v[60:63], v[232:235], v[96:99], v[60:63]\n"
      "ds_read_b64_tr_b16 v[232:233], v246 offset:25632\n"
      "ds_read_b64_tr_b16 v[234:235], v246 offset:28192\n"
      "v_mfma_f32_16x16x32_bf16 v[64:67], v[156:159], v[96:99], v[64:67]\n"
      "ds_read_b64_tr_b16 v[156:157], v246 offset:25664\n"
      "ds_read_b64_tr_b16 v[158:159], v246 offset:28224\n"
      "v_mfma_f32_16x16x32_bf16 v[68:71], v[160:163], v[96:99], v[68:71]\n"
      "ds_read_b64_tr_b16 v[160:161], v246 offset:25696\n"
      "ds_read_b64_tr_b16 v[162:163], v246 offset:28256\n"
      "v_mfma_f32_16x16x32_bf16 v[72:75], v[212:215], v[96:99], v[72:75]\n"
      "ds_read_b64_tr_b16 v[212:213], v246 offset:25728\n"
      "ds_read_b64_tr_b16 v[214:215], v246 offset:28288\n"
      "s_waitcnt vmcnt(9) lgkmcnt(15)\n"
      "v_mfma_f32_16x16x32_bf16 v[56:59], v[216:219], v[100:103], v[56:59]\n"
      "ds_read_b64_tr_b16 v[216:217], v246 offset:30720\n"
      "ds_read_b64_tr_b16 v[218:219], v246 offset:33280\n"
      "buffer_load_dwordx4 v[96:99], v238, s[36:39], s87 offen offset:1024 sc1\n"
      "v_mfma_f32_16x16x32_bf16 v[60:63], v[220:223], v[100:103], v[60:63]\n"
      "ds_read_b64_tr_b16 v[220:221], v246 offset:30752\n"
      "ds_read_b64_tr_b16 v[222:223], v246 offset:33312\n"
      "v_mfma_f32_16x16x32_bf16 v[64:67], v[224:227], v[100:103], v[64:67]\n"
      "ds_read_b64_tr_b16 v[224:225], v246 offset:30784\n"
      "ds_read_b64_tr_b16 v[226:227], v246 offset:33344\n"
      "v_mfma_f32_16x16x32_bf16 v[68:71], v[4:7], v[100:103], v[68:71]\n"
      "ds_read_b64_tr_b16 v[4:5], v246 offset:30816\n"
      "ds_read_b64_tr_b16 v[6:7], v246 offset:33376\n"
      "v_mfma_f32_16x16x32_bf16 v[72:75], v[8:11], v[100:103], v[72:75]\n"
      "ds_read_b64_tr_b16 v[8:9], v246 offset:30848\n"
      "ds_read_b64_tr_b16 v[10:11], v246 offset:33408\n"
      "s_waitcnt vmcnt(9) lgkmcnt(15)\n"
      "v_mfma_f32_16x16x32_bf16 v[56:59], v[12:15], v[104:107], v[56:59]\n"
      "ds_read_b64_tr_b16 v[12:13], v246 offset:35840\n"
      "ds_read_b64_tr_b16 v[14:15], v246 offset:38400\n"
      "buffer_load_dwordx4 v[100:103], v238, s[36:39], s87 offen offset:2048 sc1\n"
      "v_mfma_f32_16x16x32_bf16 v[60:63], v[16:19], v[104:107], v[60:63]\n"
      "ds_read_b64_tr_b16 v[16:17], v246 offset:35872\n"
      "ds_read_b64_tr_b16 v[18:19], v246 offset:38432\n"
      "v_mfma_f32_16x16x32_bf16 v[64:67], v[84:87], v[104:107], v[64:67]\n"
      "ds_read_b64_tr_b16 v[84:85], v246 offset:35904\n"
      "ds_read_b64_tr_b16 v[86:87], v246 offset:38464\n"
      "v_mfma_f32_16x16x32_bf16 v[68:71], v[180:183], v[104:107], v[68:71]\n"
      "ds_read_b64_tr_b16 v[180:181], v246 offset:35936\n"
      "ds_read_b64_tr_b16 v[182:183], v246 offset:38496\n"
      "v_mfma_f32_16x16x32_bf16 v[72:75], v[240:243], v[104:107], v[72:75]\n"
      "ds_read_b64_tr_b16 v[240:241], v246 offset:35968\n"
      "ds_read_b64_tr_b16 v[242:243], v246 offset:38528\n"
      "s_waitcnt vmcnt(9) lgkmcnt(15)\n"
      "v_mfma_f32_16x16x32_bf16 v[56:59], v[252:255], v[108:111], v[56:59]\n"
      "ds_read_b64_tr_b16 v[252:253], v246 offset:40960\n"
      "ds_read_b64_tr_b16 v[254:255], v246 offset:43520\n"
      "buffer_load_dwordx4 v[104:107], v238, s[36:39], s87 offen offset:3072 sc1\n"
      "v_mfma_f32_16x16x32_bf16 v[60:63], v[232:235], v[108:111], v[60:63]\n"
      "ds_read_b64_tr_b16 v[232:233], v246 offset:40992\n"
      "ds_read_b64_tr_b16 v[234:235], v246 offset:43552\n"
      "v_mfma_f32_16x16x32_bf16 v[64:67], v[156:159], v[108:111], v[64:67]\n"
      "ds_read_b64_tr_b16 v[156:157], v246 offset:41024\n"
      "ds_read_b64_tr_b16 v[158:159], v246 offset:43584\n"
      "v_mfma_f32_16x16x32_bf16 v[68:71], v[160:163], v[108:111], v[68:71]\n"
      "ds_read_b64_tr_b16 v[160:161], v246 offset:41056\n"
      "ds_read_b64_tr_b16 v[162:163], v246 offset:43616\n"
      "v_mfma_f32_16x16x32_bf16 v[72:75], v[212:215], v[108:111], v[72:75]\n"
      "ds_read_b64_tr_b16 v[212:213], v246 offset:41088\n"
      "ds_read_b64_tr_b16 v[214:215], v246 offset:43648\n"
      "s_waitcnt vmcnt(9) lgkmcnt(15)\n"
      "v_mfma_f32_16x16x32_bf16 v[56:59], v[216:219], v[112:115], v[56:59]\n"
      "ds_read_b64_tr_b16 v[216:217], v246 offset:46080\n"
      "ds_read_b64_tr_b16 v[218:219], v246 offset:48640\n"
      "buffer_load_dwordx4 v[108:111], v238, s[36:39], s85 offen offset:0 sc1\n"
      "v_mfma_f32_16x16x32_bf16 v[60:63], v[220:223], v[112:115], v[60:63]\n"
      "ds_read_b64_tr_b16 v[220:221], v246 offset:46112\n"
      "ds_read_b64_tr_b16 v[222:223], v246 offset:48672\n"
      "v_mfma_f32_16x16x32_bf16 v[64:67], v[224:227], v[112:115], v[64:67]\n"
      "ds_read_b64_tr_b16 v[224:225], v246 offset:46144\n"
      "ds_read_b64_tr_b16 v[226:227], v246 offset:48704\n"
      "v_mfma_f32_16x16x32_bf16 v[68:71], v[4:7], v[112:115], v[68:71]\n"
      "ds_read_b64_tr_b16 v[4:5], v246 offset:46176\n"
      "ds_read_b64_tr_b16 v[6:7], v246 offset:48736\n"
      "v_mfma_f32_16x16x32_bf16 v[72:75], v[8:11], v[112:115], v[72:75]\n"
      "ds_read_b64_tr_b16 v[8:9], v246 offset:46208\n"
      "ds_read_b64_tr_b16 v[10:11], v246 offset:48768\n"
      "s_waitcnt vmcnt(9) lgkmcnt(15)\n"
      "v_mfma_f32_16x16x32_bf16 v[56:59], v[12:15], v[116:119], v[56:59]\n"
      "ds_read_b64_tr_b16 v[12:13], v246 offset:51200\n"
      "ds_read_b64_tr_b16 v[14:15], v246 offset:53760\n"
      "buffer_load_dwordx4 v[112:115], v238, s[36:39], s85 offen offset:1024 sc1\n"
      "v_mfma_f32_16x16x32_bf16 v[60:63], v[16:19], v[116:119], v[60:63]\n"
      "ds_read_b64_tr_b16 v[16:17], v246 offset:51232\n"
      "ds_read_b64_tr_b16 v[18:19], v246 offset:53792\n"
      "v_mfma_f32_16x16x32_bf16 v[64:67], v[84:87], v[116:119], v[64:67]\n"
      "ds_read_b64_tr_b16 v[84:85], v246 offset:51264\n"
      "ds_read_b64_tr_b16 v[86:87], v246 offset:53824\n"
      "v_mfma_f32_16x16x32_bf16 v[68:71], v[180:183], v[116:119], v[68:71]\n"
      "ds_read_b64_tr_b16 v[180:181], v246 offset:51296\n"
      "ds_read_b64_tr_b16 v[182:183], v246 offset:53856\n"
      "v_mfma_f32_16x16x32_bf16 v[72:75], v[240:243], v[116:119], v[72:75]\n"
      "ds_read_b64_tr_b16 v[240:241], v246 offset:51328\n"
      "ds_read_b64_tr_b16 v[242:243], v246 offset:53888\n"
      "s_waitcnt vmcnt(9) lgkmcnt(15)\n"
      "v_mfma_f32_16x16x32_bf16 v[56:59], v[252:255], v[120:123], v[56:59]\n"
      "ds_read_b64_tr_b16 v[252:253], v246 offset:56320\n"
      "ds_read_b64_tr_b16 v[254:255], v246 offset:58880\n"
      "buffer_load_dwordx4 v[116:119], v238, s[36:39], s85 offen offset:2048 sc1\n"
      "v_mfma_f32_16x16x32_bf16 v[60:63], v[232:235], v[120:123], v[60:63]\n"
      "ds_read_b64_tr_b16 v[232:233], v246 offset:56352\n"
      "ds_read_b64_tr_b16 v[234:235], v246 offset:58912\n"
      "v_mfma_f32_16x16x32_bf16 v[64:67], v[156:159], v[120:123], v[64:67]\n"
      "ds_read_b64_tr_b16 v[156:157], v246 offset:56384\n"
      "ds_read_b64_tr_b16 v[158:159], v246 offset:58944\n"
      "v_mfma_f32_16x16x32_bf16 v[68:71], v[160:163], v[120:123], v[68:71]\n"
      "ds_read_b64_tr_b16 v[160:161], v246 offset:56416\n"
      "ds_read_b64_tr_b16 v[162:163], v246 offset:58976\n"
      "v_mfma_f32_16x16x32_bf16 v[72:75], v[212:215], v[120:123], v[72:75]\n"
      "ds_read_b64_tr_b16 v[212:213], v246 offset:56448\n"
      "ds_read_b64_tr_b16 v[214:215], v246 offset:59008\n"
      "s_waitcnt vmcnt(9) lgkmcnt(15)\n"
      "v_mfma_f32_16x16x32_bf16 v[56:59], v[216:219], v[124:127], v[56:59]\n"
      "ds_read_b64_tr_b16 v[216:217], v247 offset:0\n"
      "ds_read_b64_tr_b16 v[218:219], v247 offset:2560\n"
      "buffer_load_dwordx4 v[120:123], v238, s[36:39], s85 offen offset:3072 sc1\n"
      "v_mfma_f32_16x16x32_bf16 v[60:63], v[220:223], v[124:127], v[60:63]\n"
      "ds_read_b64_tr_b16 v[220:221], v247 offset:32\n"
      "ds_read_b64_tr_b16 v[222:223], v247 offset:2592\n"
      "v_mfma_f32_16x16x32_bf16 v[64:67], v[224:227], v[124:127], v[64:67]\n"
      "ds_read_b64_tr_b16 v[224:225], v247 offset:64\n"
      "ds_read_b64_tr_b16 v[226:227], v247 offset:2624\n"
      "v_mfma_f32_16x16x32_bf16 v[68:71], v[4:7], v[124:127], v[68:71]\n"
      "ds_read_b64_tr_b16 v[4:5], v247 offset:96\n"
      "ds_read_b64_tr_b16 v[6:7], v247 offset:2656\n"
      "buffer_store_dwordx2 v[166:167], v231, s[36:39], 0 offen sc1\n"
      "v_mfma_f32_16x16x32_bf16 v[72:75], v[8:11], v[124:127], v[72:75]\n"
      "ds_read_b64_tr_b16 v[8:9], v247 offset:128\n"
      "ds_read_b64_tr_b16 v[10:11], v247 offset:2688\n"
      "s_waitcnt vmcnt(10) lgkmcnt(15)\n"
      "v_mfma_f32_16x16x32_bf16 v[56:59], v[12:15], v[128:131], v[56:59]\n"
      "ds_read_b64_tr_b16 v[12:13], v247 offset:5120\n"
      "ds_read_b64_tr_b16 v[14:15], v247 offset:7680\n"
      "s_add_u32 m0, s82, 114688\n"
      "s_add_u32 s94, s76, 1769472\n"
      "buffer_load_dwordx4 v185, s[20:23], s94 offen offset:0 lds\n"
      "v_mfma_f32_16x16x32_bf16 v[60:63], v[16:19], v[128:131], v[60:63]\n"
      "ds_read_b64_tr_b16 v[16:17], v247 offset:5152\n"
      "ds_read_b64_tr_b16 v[18:19], v247 offset:7712\n"
      "v_mfma_f32_16x16x32_bf16 v[64:67], v[84:87], v[128:131], v[64:67]\n"
      "ds_read_b64_tr_b16 v[84:85], v247 offset:5184\n"
      "ds_read_b64_tr_b16 v[86:87], v247 offset:7744\n"
      "v_mfma_f32_16x16x32_bf16 v[68:71], v[180:183], v[128:131], v[68:71]\n"
      "ds_read_b64_tr_b16 v[180:181], v247 offset:5216\n"
      "ds_read_b64_tr_b16 v[182:183], v247 offset:7776\n"
      "buffer_load_dwordx4 v177, s[20:23], s94 offen offset:1024 lds\n"
      "v_mfma_f32_16x16x32_bf16 v[72:75], v[240:243], v[128:131], v[72:75]\n"
      "ds_read_b64_tr_b16 v[240:241], v247 offset:5248\n"
      "ds_read_b64_tr_b16 v[242:243], v247 offset:7808\n"
      "s_waitcnt vmcnt(11) lgkmcnt(15)\n"
      "v_mfma_f32_16x16x32_bf16 v[56:59], v[252:255], v[88:91], v[56:59]\n"
      "ds_read_b64_tr_b16 v[252:253], v247 offset:10240\n"
      "ds_read_b64_tr_b16 v[254:255], v247 offset:12800\n"
      "buffer_load_dwordx4 v200, s[20:23], s94 offen offset:2048 lds\n"
      "v_mfma_f32_16x16x32_bf16 v[60:63], v[232:235], v[88:91], v[60:63]\n"
      "ds_read_b64_tr_b16 v[232:233], v247 offset:10272\n"
      "ds_read_b64_tr_b16 v[234:235], v247 offset:12832\n"
      "v_mfma_f32_16x16x32_bf16 v[64:67], v[156:159], v[88:91], v[64:67]\n"
      "ds_read_b64_tr_b16 v[156:157], v247 offset:10304\n"
      "ds_read_b64_tr_b16 v[158:159], v247 offset:12864\n"
      "v_mfma_f32_16x16x32_bf16 v[68:71], v[160:163], v[88:91], v[68:71]\n"
      "ds_read_b64_tr_b16 v[160:161], v247 offset:10336\n"
      "ds_read_b64_tr_b16 v[162:163], v247 offset:12896\n"
      "buffer_load_dwordx4 v173, s[20:23], s94 offen offset:3072 lds\n"
      "v_mfma_f32_16x16x32_bf16 v[72:75], v[212:215], v[88:91], v[72:75]\n"
      "ds_read_b64_tr_b16 v[212:213], v247 offset:10368\n"
      "ds_read_b64_tr_b16 v[214:215], v247 offset:12928\n"
      "s_waitcnt vmcnt(12) lgkmcnt(15)\n"
      "v_mfma_f32_16x16x32_bf16 v[56:59], v[216:219], v[92:95], v[56:59]\n"
      "ds_read_b64_tr_b16 v[216:217], v247 offset:15360\n"
      "ds_read_b64_tr_b16 v[218:219], v247 offset:17920\n"
      "s_add_u32 m0, s82, 118784\n"
      "s_nop 0\n"
      "buffer_load_dwordx4 v172, s[20:23], s94 offen offset:0 lds\n"
      "v_mfma_f32_16x16x32_bf16 v[60:63], v[220:223], v[92:95], v[60:63]\n"
      "ds_read_b64_tr_b16 v[220:221], v247 offset:15392\n"
      "ds_read_b64_tr_b16 v[222:223], v247 offset:17952\n"
      "v_mfma_f32_16x16x32_bf16 v[64:67], v[224:227], v[92:95], v[64:67]\n"
      "ds_read_b64_tr_b16 v[224:225], v247 offset:15424\n"
      "ds_read_b64_tr_b16 v[226:227], v247 offset:17984\n"
      "v_mfma_f32_16x16x32_bf16 v[68:71], v[4:7], v[92:95], v[68:71]\n"
      "ds_read_b64_tr_b16 v[4:5], v247 offset:15456\n"
      "ds_read_b64_tr_b16 v[6:7], v247 offset:18016\n"
      "buffer_load_dwordx4 v169, s[20:23], s94 offen offset:1024 lds\n"
      "v_mfma_f32_16x16x32_bf16 v[72:75], v[8:11], v[92:95], v[72:75]\n"
      "ds_read_b64_tr_b16 v[8:9], v247 offset:15488\n"
      "ds_read_b64_tr_b16 v[10:11], v247 offset:18048\n"
      "s_waitcnt vmcnt(13) lgkmcnt(15)\n"
      "v_mfma_f32_16x16x32_bf16 v[56:59], v[12:15], v[96:99], v[56:59]\n"
      "ds_read_b64_tr_b16 v[12:13], v247 offset:20480\n"
      "ds_read_b64_tr_b16 v[14:15], v247 offset:23040\n"
      "buffer_load_dwordx4 v168, s[20:23], s94 offen offset:2048 lds\n"
      "v_mfma_f32_16x16x32_bf16 v[60:63], v[16:19], v[96:99], v[60:63]\n"
      "ds_read_b64_tr_b16 v[16:17], v247 offset:20512\n"
      "ds_read_b64_tr_b16 v[18:19], v247 offset:23072\n"
      "v_mfma_f32_16x16x32_bf16 v[64:67], v[84:87], v[96:99], v[64:67]\n"
      "ds_read_b64_tr_b16 v[84:85], v247 offset:20544\n"
      "ds_read_b64_tr_b16 v[86:87], v247 offset:23104\n"
      "v_mfma_f32_16x16x32_bf16 v[68:71], v[180:183], v[96:99], v[68:71]\n"
      "ds_read_b64_tr_b16 v[180:181], v247 offset:20576\n"
      "ds_read_b64_tr_b16 v[182:183], v247 offset:23136\n"
      "buffer_load_dwordx4 a[192:195], v239, s[36:39], s96 offen offset:1024 sc1\n"
      "v_mfma_f32_16x16x32_bf16 v[72:75], v[240:243], v[96:99], v[72:75]\n"
      "ds_read_b64_tr_b16 v[240:241], v247 offset:20608\n"
      "ds_read_b64_tr_b16 v[242:243], v247 offset:23168\n"
      "s_waitcnt vmcnt(14) lgkmcnt(15)\n"
      "v_mfma_f32_16x16x32_bf16 v[56:59], v[252:255], v[100:103], v[56:59]\n"
      "ds_read_b64_tr_b16 v[252:253], v247 offset:25600\n"
      "ds_read_b64_tr_b16 v[254:255], v247 offset:28160\n"
      "s_mov_b32 s96, 14336\n"
      "buffer_load_dwordx4 a[196:199], v239, s[36:39], s96 offen offset:0 sc1\n"
      "v_mfma_f32_16x16x32_bf16 v[60:63], v[232:235], v[100:103], v[60:63]\n"
      "ds_read_b64_tr_b16 v[232:233], v247 offset:25632\n"
      "ds_read_b64_tr_b16 v[234:235], v247 offset:28192\n"
      "v_mfma_f32_16x16x32_bf16 v[64:67], v[156:159], v[100:103], v[64:67]\n"
      "ds_read_b64_tr_b16 v[156:157], v247 offset:25664\n"
      "ds_read_b64_tr_b16 v[158:159], v247 offset:28224\n"
      "v_mfma_f32_16x16x32_bf16 v[68:71], v[160:163], v[100:103], v[68:71]\n"
      "ds_read_b64_tr_b16 v[160:161], v247 offset:25696\n"
      "ds_read_b64_tr_b16 v[162:163], v247 offset:28256\n"
      "s_add_u32 s96, s83, 10240\n"
      "buffer_load_dwordx4 a[200:203], v239, s[36:39], s96 offen offset:0 sc1\n"
      "v_mfma_f32_16x16x32_bf16 v[72:75], v[212:215], v[100:103], v[72:75]\n"
      "ds_read_b64_tr_b16 v[212:213], v247 offset:25728\n"
      "ds_read_b64_tr_b16 v[214:215], v247 offset:28288\n"
      "s_waitcnt vmcnt(15) lgkmcnt(15)\n"
      "v_mfma_f32_16x16x32_bf16 v[56:59], v[216:219], v[104:107], v[56:59]\n"
      "ds_read_b64_tr_b16 v[216:217], v247 offset:30720\n"
      "ds_read_b64_tr_b16 v[218:219], v247 offset:33280\n"
      "buffer_load_dwordx4 a[204:207], v239, s[36:39], s96 offen offset:1024 sc1\n"
      "v_mfma_f32_16x16x32_bf16 v[60:63], v[220:223], v[104:107], v[60:63]\n"
      "ds_read_b64_tr_b16 v[220:221], v247 offset:30752\n"
      "ds_read_b64_tr_b16 v[222:223], v247 offset:33312\n"
      "v_mfma_f32_16x16x32_bf16 v[64:67], v[224:227], v[104:107], v[64:67]\n"
      "ds_read_b64_tr_b16 v[224:225], v247 offset:30784\n"
      "ds_read_b64_tr_b16 v[226:227], v247 offset:33344\n"
      "v_mfma_f32_16x16x32_bf16 v[68:71], v[4:7], v[104:107], v[68:71]\n"
      "ds_read_b64_tr_b16 v[4:5], v247 offset:30816\n"
      "ds_read_b64_tr_b16 v[6:7], v247 offset:33376\n"
      "s_add_u32 s96, s97, 15360\n"
      "buffer_load_dwordx4 a[208:211], v239, s[36:39], s96 offen offset:0 sc1\n"
      "v_mfma_f32_16x16x32_bf16 v[72:75], v[8:11], v[104:107], v[72:75]\n"
      "ds_read_b64_tr_b16 v[8:9], v247 offset:30848\n"
      "ds_read_b64_tr_b16 v[10:11], v247 offset:33408\n"
      "s_waitcnt vmcnt(16) lgkmcnt(15)\n"
      "v_mfma_f32_16x16x32_bf16 v[56:59], v[12:15], v[108:111], v[56:59]\n"
      "ds_read_b64_tr_b16 v[12:13], v247 offset:35840\n"
      "ds_read_b64_tr_b16 v[14:15], v247 offset:38400\n"
      "buffer_load_dwordx4 a[212:215], v239, s[36:39], s96 offen offset:1024 sc1\n"
      "v_mfma_f32_16x16x32_bf16 v[60:63], v[16:19], v[108:111], v[60:63]\n"
      "ds_read_b64_tr_b16 v[16:17], v247 offset:35872\n"
      "ds_read_b64_tr_b16 v[18:19], v247 offset:38432\n"
      "v_mfma_f32_16x16x32_bf16 v[64:67], v[84:87], v[108:111], v[64:67]\n"
      "ds_read_b64_tr_b16 v[84:85], v247 offset:35904\n"
      "ds_read_b64_tr_b16 v[86:87], v247 offset:38464\n"
      "v_mfma_f32_16x16x32_bf16 v[68:71], v[180:183], v[108:111], v[68:71]\n"
      "ds_read_b64_tr_b16 v[180:181], v247 offset:35936\n"
      "ds_read_b64_tr_b16 v[182:183], v247 offset:38496\n"
      "s_mov_b32 s96, 19456\n"
      "buffer_load_dwordx4 a[216:219], v239, s[36:39], s96 offen offset:0 sc1\n"
      "v_mfma_f32_16x16x32_bf16 v[72:75], v[240:243], v[108:111], v[72:75]\n"
      "ds_read_b64_tr_b16 v[240:241], v247 offset:35968\n"
      "ds_read_b64_tr_b16 v[242:243], v247 offset:38528\n"
      "s_waitcnt vmcnt(17) lgkmcnt(15)\n"
      "v_mfma_f32_16x16x32_bf16 v[56:59], v[252:255], v[112:115], v[56:59]\n"
      "s_add_u32 s96, s83, 15360\n"
      "buffer_load_dwordx4 a[220:223], v239, s[36:39], s96 offen offset:0 sc1\n"
      "v_mfma_f32_16x16x32_bf16 v[60:63], v[232:235], v[112:115], v[60:63]\n"
      "v_mfma_f32_16x16x32_bf16 v[64:67], v[156:159], v[112:115], v[64:67]\n"
      "v_mfma_f32_16x16x32_bf16 v[68:71], v[160:163], v[112:115], v[68:71]\n"
      "buffer_load_dwordx4 a[224:227], v239, s[36:39], s96 offen offset:1024 sc1\n"
      "v_mfma_f32_16x16x32_bf16 v[72:75], v[212:215], v[112:115], v[72:75]\n"
      "s_waitcnt vmcnt(18) lgkmcnt(10)\n"
      "v_mfma_f32_16x16x32_bf16 v[56:59], v[216:219], v[116:119], v[56:59]\n"
      "v_mfma_f32_16x16x32_bf16 v[60:63], v[220:223], v[116:119], v[60:63]\n"
      "v_mfma_f32_16x16x32_bf16 v[64:67], v[224:227], v[116:119], v[64:67]\n"
      "v_mfma_f32_16x16x32_bf16 v[68:71], v[4:7], v[116:119], v[68:71]\n"
      "v_mfma_f32_16x16x32_bf16 v[72:75], v[8:11], v[116:119], v[72:75]\n"
      "s_waitcnt vmcnt(17) lgkmcnt(0)\n"
      "v_mfma_f32_16x16x32_bf16 v[56:59], v[12:15], v[120:123], v[56:59]\n"
      "v_mfma_f32_16x16x32_bf16 v[60:63], v[16:19], v[120:123], v[60:63]\n"
      "v_mfma_f32_16x16x32_bf16 v[64:67], v[84:87], v[120:123], v[64:67]\n"
      "v_mfma_f32_16x16x32_bf16 v[68:71], v[180:183], v[120:123], v[68:71]\n"
      "v_mfma_f32_16x16x32_bf16 v[72:75], v[240:243], v[120:123], v[72:75]\n"
      "s_nop 3\n"
      "v_cvt_pk_bf16_f32 v88, v56, v57\n"
      "v_cvt_pk_bf16_f32 v89, v58, v59\n"
      "v_cvt_pk_bf16_f32 v90, v60, v61\n"
      "v_cvt_pk_bf16_f32 v91, v62, v63\n"
      "buffer_store_dwordx4 v[88:91], v195, s[36:39], 0 offen sc1\n"
      "v_cvt_pk_bf16_f32 v92, v64, v65\n"
      "v_cvt_pk_bf16_f32 v93, v66, v67\n"
      "v_cvt_pk_bf16_f32 v94, v68, v69\n"
      "v_cvt_pk_bf16_f32 v95, v70, v71\n"
      "buffer_store_dwordx4 v[92:95], v195, s[36:39], 0 offen offset:1024 sc1\n"
      "v_cvt_pk_bf16_f32 v96, v72, v73\n"
      "v_cvt_pk_bf16_f32 v97, v74, v75\n"
      "v_dot2_f32_bf16 v100, v96, v196, 0\n"
      "v_dot2_f32_bf16 v101, v96, v96, 0\n"
      "v_dot2_f32_bf16 v100, v97, v196, v100\n"
      "v_dot2_f32_bf16 v101, v97, v97, v101\n"
      "v_dot2_f32_bf16 v100, v88, v196, v100\n"
      "v_dot2_f32_bf16 v101, v88, v88, v101\n"
      "v_dot2_f32_bf16 v100, v89, v196, v100\n"
      "v_dot2_f32_bf16 v101, v89, v89, v101\n"
      "v_permlane32_swap_b32 v96, v98\n"
      "v_dot2_f32_bf16 v100, v90, v196, v100\n"
      "v_dot2_f32_bf16 v101, v90, v90, v101\n"
      "v_permlane32_swap_b32 v97, v99\n"
      "v_dot2_f32_bf16 v100, v91, v196, v100\n"
      "v_dot2_f32_bf16 v101, v91, v91, v101\n"
      "buffer_store_dwordx4 v[96:99], v190, s[36:39], 0 offen sc1\n"
      "v_dot2_f32_bf16 v100, v92, v196, v100\n"
      "v_dot2_f32_bf16 v101, v92, v92, v101\n"
      "v_dot2_f32_bf16 v100, v93, v196, v100\n"
      "v_dot2_f32_bf16 v101, v93, v93, v101\n"
      "v_dot2_f32_bf16 v100, v94, v196, v100\n"
      "v_dot2_f32_bf16 v101, v94, v94, v101\n"
      "v_dot2_f32_bf16 v100, v95, v196, v100\n"
      "v_dot2_f32_bf16 v101, v95, v95, v101\n"
      "s_nop 2\n"
      "v_permlane32_swap_b32 v100, v101\n"
      "v_add_f32 v100, v100, v101\n"
      "v_mov_b32 v102, v100\n"
      "s_nop 1\n"
      "v_permlane16_swap_b32 v100, v102\n"
      "v_add_f32 v100, v100, v102\n"
      "v_mov_b32 v101, v100\n"
      "s_nop 1\n"
      "v_permlane32_swap_b32 v100, v101\n"
      "buffer_store_dwordx2 v[100:101], v153, s[36:39], 0 offen sc1\n"
      "s_cmp_eq_u32 s101, 1\n"
      "s_cselect_b32 s94, 1792, 0\n"
      "v_lshrrev_b32 v244, 5, v1\n"
      "v_mad_u32_u24 v248, v244, s94, v230\n"
      "v_subrev_u32 v244, s94, v248\n"
      "s_waitcnt vmcnt(13)\n"
      "ds_read_b64_tr_b16 v[216:217], v248 offset:28672\n"
      "ds_read_b64_tr_b16 v[218:219], v244 offset:32256\n"
      "ds_read_b64_tr_b16 v[220:221], v248 offset:28704\n"
      "ds_read_b64_tr_b16 v[222:223], v244 offset:32288\n"
      "ds_read_b64_tr_b16 v[224:225], v248 offset:28736\n"
      "ds_read_b64_tr_b16 v[226:227], v244 offset:32320\n"
      "ds_read_b64_tr_b16 v[4:5], v248 offset:28768\n"
      "ds_read_b64_tr_b16 v[6:7], v244 offset:32352\n"
      "ds_read_b64_tr_b16 v[8:9], v248 offset:28800\n"
      "ds_read_b64_tr_b16 v[10:11], v244 offset:32384\n"
      "ds_read_b64_tr_b16 v[12:13], v248 offset:28832\n"
      "ds_read_b64_tr_b16 v[14:15], v244 offset:32416\n"
      "ds_read_b64_tr_b16 v[16:17], v248 offset:28864\n"
      "ds_read_b64_tr_b16 v[18:19], v244 offset:32448\n"
      "s_waitcnt lgkmcnt(0)\n"
      "s_barrier\n"
      "ds_write_b128 v175, a[0:3] offset:0\n"
      "v_mfma_f32_16x16x32_bf16 v[204:207], v[196:199], v[216:219], v[204:207]\n"
      "v_mfma_f32_16x16x32_bf16 v[208:211], v[196:199], v[220:223], v[208:211]\n"
      "ds_write_b128 v175, a[4:7] offset:1024\n"
      "v_mfma_f32_16x16x32_bf16 v[44:47], v[196:199], v[224:227], v[44:47]\n"
      "v_mfma_f32_16x16x32_bf16 v[48:51], v[196:199], v[4:7], v[48:51]\n"
      "ds_write_b128 v175, a[8:11] offset:2048\n"
      "v_mfma_f32_16x16x32_bf16 v[52:55], v[196:199], v[8:11], v[52:55]\n"
      "v_mfma_f32_16x16x32_bf16 v[76:79], v[196:199], v[12:15], v[76:79]\n"
      "ds_write_b128 v175, a[12:15] offset:3072\n"
      "v_mfma_f32_16x16x32_bf16 v[80:83], v[196:199], v[16:19], v[80:83]\n"
      "s_nop 0\n"
      "ds_write_b128 v175, a[16:19] offset:4096\n"
      "s_nop 0\n"
      "v_cvt_pk_bf16_f32 v205, v204, v204\n"
      "ds_write_b128 v175, a[20:23] offset:5120\n"
      "v_cvt_pk_bf16_f32 v209, v208, v208\n"
      "v_cvt_pk_bf16_f32 v45, v44, v44\n"
      "ds_write_b128 v175, a[24:27] offset:6144\n"
      "v_cvt_pk_bf16_f32 v49, v48, v48\n"
      "v_cvt_pk_bf16_f32 v53, v52, v52\n"
      "ds_write_b128 v175, a[28:31] offset:28672\n"
      "v_cvt_pk_bf16_f32 v77, v76, v76\n"
      "v_cvt_pk_bf16_f32 v81, v80, v80\n"
      "s_waitcnt vmcnt(0)\n"
      "buffer_store_dwordx2 v[166:167], v251, s[36:39], 0 offen sc1\n"
      "ds_write_b128 v175, a[32:35] offset:29696\n"
      "v_and_b32 v205, 0xffff0000, v205\n"
      "v_and_b32 v209, 0xffff0000, v209\n"
      "s_add_u32 s96, s97, 20480\n"
      "buffer_load_dwordx4 a[228:231], v239, s[36:39], s96 offen offset:0 sc1\n"
      "ds_write_b128 v175, a[36:39] offset:30720\n"
      "v_and_b32 v45, 0xffff0000, v45\n"
      "v_and_b32 v49, 0xffff0000, v49\n"
      "buffer_load_dwordx4 a[232:235], v239, s[36:39], s96 offen offset:1024 sc1\n"
      "ds_write_b128 v175, a[40:43] offset:31744\n"
      "v_and_b32 v53, 0xffff0000, v53\n"
      "v_and_b32 v77, 0xffff0000, v77\n"
      "s_mov_b32 s96, 24576\n"
      "buffer_load_dwordx4 a[236:239], v239, s[36:39], s96 offen offset:0 sc1\n"
      "ds_write_b128 v175, a[44:47] offset:32768\n"
      "v_and_b32 v81, 0xffff0000, v81\n"
      "v_sub_f32 v205, v204, v205\n"
      "s_add_u32 s96, s83, 20480\n"
      "buffer_load_dwordx4 a[240:243], v239, s[36:39], s96 offen offset:0 sc1\n"
      "ds_write_b128 v175, a[48:51] offset:33792\n"
      "v_sub_f32 v209, v208, v209\n"
      "v_sub_f32 v45, v44, v45\n"
      "buffer_load_dwordx4 a[244:247], v239, s[36:39], s96 offen offset:1024 sc1\n"
      "ds_write_b128 v175, a[52:55] offset:34816\n"
      "v_sub_f32 v49, v48, v49\n"
      "v_sub_f32 v53, v52, v53\n"
      "s_add_u32 s96, s97, 25600\n"
      "buffer_load_dwordx4 a[248:251], v239, s[36:39], s96 offen offset:0 sc1\n"
      "ds_write_b128 v175, a[56:59] offset:57344\n"
      "v_sub_f32 v77, v76, v77\n"
      "v_sub_f32 v81, v80, v81\n"
      "buffer_load_dwordx4 a[252:255], v239, s[36:39], s96 offen offset:1024 sc1\n"
      "ds_write_b128 v175, a[60:63] offset:58368\n"
      "v_cvt_pk_bf16_f32 v205, v204, v205\n"
      "v_cvt_pk_bf16_f32 v209, v208, v209\n"
      "s_mov_b32 s96, 29696\n"
      "buffer_load_dwordx4 a[84:87], v239, s[36:39], s96 offen offset:0 sc1\n"
      "ds_write_b128 v175, a[64:67] offset:59392\n"
      "v_cvt_pk_bf16_f32 v45, v44, v45\n"
      "v_cvt_pk_bf16_f32 v49, v48, v49\n"
      "s_add_u32 s96, s83, 25600\n"
      "buffer_load_dwordx4 a[88:91], v239, s[36:39], s96 offen offset:0 sc1\n"
      "ds_write_b128 v175, a[68:71] offset:60416\n"
      "v_cvt_pk_bf16_f32 v53, v52, v53\n"
      "v_cvt_pk_bf16_f32 v77, v76, v77\n"
      "buffer_load_dwordx4 a[92:95], v239, s[36:39], s96 offen offset:1024 sc1\n"
      "ds_write_b128 v175, a[72:75] offset:61440\n"
      "v_cvt_pk_bf16_f32 v81, v80, v81\n"
      "s_add_u32 s96, s97, 30720\n"
      "buffer_load_dwordx4 a[96:99], v239, s[36:39], s96 offen offset:0 sc1\n"
      "ds_write_b128 v175, a[76:79] offset:62464\n"
      "buffer_load_dwordx4 a[100:103], v239, s[36:39], s96 offen offset:1024 sc1\n"
      "ds_write_b128 v175, a[80:83] offset:63488\n"
      "s_mov_b32 s96, 34816\n"
      "buffer_load_dwordx4 a[104:107], v239, s[36:39], s96 offen offset:0 sc1\n"
      "ds_write_b128 v193, v[216:219] offset:28672\n"
      "s_add_u32 s96, s83, 30720\n"
      "buffer_load_dwordx4 a[108:111], v239, s[36:39], s96 offen offset:0 sc1\n"
      "ds_write_b128 v193, v[220:223] offset:29696\n"
      "buffer_load_dwordx4 a[112:115], v239, s[36:39], s96 offen offset:1024 sc1\n"
      "ds_write_b128 v193, v[224:227] offset:30720\n"
      "s_add_u32 s96, s97, 35840\n"
      "buffer_load_dwordx4 a[116:119], v239, s[36:39], s96 offen offset:0 sc1\n"
      "ds_write_b128 v193, v[4:7] offset:31744\n"
      "buffer_load_dwordx4 a[120:123], v239, s[36:39], s96 offen offset:1024 sc1\n"
      "ds_write_b128 v193, v[8:11] offset:32768\n"
      "s_mov_b32 s96, 39936\n"
      "buffer_load_dwordx4 a[124:127], v239, s[36:39], s96 offen offset:0 sc1\n"
      "ds_write_b128 v193, v[12:15] offset:33792\n"
      "s_add_u32 s96, s83, 35840\n"
      "buffer_load_dwordx4 a[128:131], v239, s[36:39], s96 offen offset:0 sc1\n"
      "ds_write_b128 v193, v[16:19] offset:34816\n"
      "buffer_load_dwordx4 a[132:135], v239, s[36:39], s96 offen offset:1024 sc1\n"
      "s_mov_b64 exec, 0xffff\n"
      "ds_write_b32 v188, v205 offset:0\n"
      "ds_write_b32 v188, v209 offset:256\n"
      "ds_write_b32 v188, v45 offset:512\n"
      "ds_write_b32 v188, v49 offset:768\n"
      "ds_write_b32 v188, v53 offset:1024\n"
      "ds_write_b32 v188, v77 offset:1280\n"
      "ds_write_b32 v188, v81 offset:1536\n"
      "s_mov_b64 exec, -1\n"
      "s_waitcnt lgkmcnt(0)\n"
      "s_barrier\n"
      "ds_read_b128 v[180:183], v3 offset:20480\n"
      "ds_read_b128 v[240:243], v3 offset:21504\n"
      "ds_read_b128 v[252:255], v3 offset:22528\n"
      "ds_read_b128 v[232:235], v3 offset:23552\n"
      "ds_read_b128 v[156:159], v3 offset:24576\n"
      "ds_read_b128 v[160:163], v3 offset:25600\n"
      "ds_read_b128 v[212:215], v3 offset:26624\n"
      "ds_read_b128 v[216:219], v3 offset:27648\n"
      "ds_read_b128 v[220:223], v3 offset:28672\n"
      "ds_read_b128 v[224:227], v3 offset:29696\n"
      "ds_read_b128 v[4:7], v3 offset:30720\n"
      "ds_read_b128 v[8:11], v3 offset:31744\n"
      "ds_read_b128 v[12:15], v3 offset:32768\n"
      "ds_read_b128 v[16:19], v3 offset:33792\n"
      "s_mov_b32 s94, 200000\n"
      "s_mov_b64 exec, 0xffff\n"
      "s_branch L14_%=\n"
      "L12_%=:\n"
      "s_sleep 1\n"
      "L14_%=:\n"
      "buffer_load_dwordx2 v[246:247], v250, s[36:39], 0 offen sc1\n"
      "s_waitcnt vmcnt(0)\n"
      "v_cmp_ne_u64 vcc, v[246:247], v[166:167]\n"
      "s_sub_u32 s94, s94, 1\n"
      "s_cbranch_scc1 L13_%=\n"
      "s_cbranch_vccnz L12_%=\n"
      "L13_%=:\n"
      "s_mov_b64 exec, -1\n"
      "buffer_load_dwordx4 v[192:195], v184, s[36:39], 0 offen offset:0 sc1\n"
      "buffer_load_dwordx4 v[196:199], v184, s[36:39], 0 offen offset:1024 sc1\n"
      "buffer_load_dwordx4 v[84:87], v184, s[36:39], 0 offen offset:2048 sc1\n"
      "buffer_load_dwordx4 v[88:91], v184, s[36:39], 0 offen offset:3072 sc1\n"
      "buffer_load_dwordx2 v[168:169], v245, s[36:39], 0 offen offset:0 sc1\n"
      "buffer_load_dwordx2 v[170:171], v245, s[36:39], 0 offen offset:128 sc1\n"
      "buffer_load_dwordx2 v[188:189], v245, s[36:39], 0 offen offset:1024 sc1\n"
      "buffer_load_dwordx2 v[190:191], v245, s[36:39], 0 offen offset:1152 sc1\n"
      "buffer_load_dwordx4 v[92:95], v184, s[36:39], s98 offen offset:0 sc1\n"
      "buffer_load_dwordx4 v[96:99], v184, s[36:39], s98 offen offset:1024 sc1\n"
      "buffer_load_dwordx4 v[100:103], v184, s[36:39], s98 offen offset:2048 sc1\n"
      "buffer_load_dwordx4 v[104:107], v184, s[36:39], s98 offen offset:3072 sc1\n"
      "buffer_load_dwordx4 v[108:111], v184, s[36:39], s90 offen offset:0 sc1\n"
      "s_mov_b64 s[92:93], 0xffff\n"
      "s_mov_b32 s80, -1\n"
      "s_mov_b32 s81, 0\n"
      "s_waitcnt vmcnt(12) lgkmcnt(7)\n"
      "v_mfma_f32_16x16x32_bf16 v[112:115], v[180:183], v[192:195], 0\n"
      "ds_read_b128 v[180:183], v3 offset:34816\n"
      "v_mfma_f32_16x16x32_bf16 v[116:119], v[240:243], v[192:195], 0\n"
      "ds_read_b128 v[240:243], v3 offset:35840\n"
      "v_mfma_f32_16x16x32_bf16 v[120:123], v[252:255], v[192:195], 0\n"
      "ds_read_b128 v[252:255], v3 offset:36864\n"
      "v_mfma_f32_16x16x32_bf16 v[124:127], v[232:235], v[192:195], 0\n"
      "ds_read_b128 v[232:235], v3 offset:37888\n"
      "buffer_load_dwordx4 a[4:7], v176, s[36:39], 0 offen offset:0 sc1\n"
      "v_mfma_f32_16x16x32_bf16 v[128:131], v[156:159], v[192:195], 0\n"
      "ds_read_b128 v[156:159], v3 offset:38912\n"
      "v_mfma_f32_16x16x32_bf16 v[132:135], v[160:163], v[192:195], 0\n"
      "ds_read_b128 v[160:163], v3 offset:39936\n"
      "v_mfma_f32_16x16x32_bf16 v[136:139], v[212:215], v[192:195], 0\n"
      "ds_read_b128 v[212:215], v3 offset:40960\n"
      "s_waitcnt vmcnt(12) lgkmcnt(7)\n"
      "v_mfma_f32_16x16x32_bf16 v[112:115], v[216:219], v[196:199], v[112:115]\n"
      "ds_read_b128 v[216:219], v3 offset:41984\n"
      "buffer_load_dwordx4 v[192:195], v184, s[36:39], s90 offen offset:1024 sc1\n"
      "v_mfma_f32_16x16x32_bf16 v[116:119], v[220:223], v[196:199], v[116:119]\n"
      "ds_read_b128 v[220:223], v3 offset:43008\n"
      "v_mfma_f32_16x16x32_bf16 v[120:123], v[224:227], v[196:199], v[120:123]\n"
      "ds_read_b128 v[224:227], v3 offset:44032\n"
      "v_mfma_f32_16x16x32_bf16 v[124:127], v[4:7], v[196:199], v[124:127]\n"
      "ds_read_b128 v[4:7], v3 offset:45056\n"
      "buffer_load_dwordx4 a[8:11], v176, s[36:39], 0 offen offset:1024 sc1\n"
      "v_mfma_f32_16x16x32_bf16 v[128:131], v[8:11], v[196:199], v[128:131]\n"
      "ds_read_b128 v[8:11], v3 offset:46080\n"
      "v_mfma_f32_16x16x32_bf16 v[132:135], v[12:15], v[196:199], v[132:135]\n"
      "ds_read_b128 v[12:15], v3 offset:47104\n"
      "v_mfma_f32_16x16x32_bf16 v[136:139], v[16:19], v[196:199], v[136:139]\n"
      "ds_read_b128 v[16:19], v3 offset:48128\n"
      "s_waitcnt vmcnt(13) lgkmcnt(7)\n"
      "v_mfma_f32_16x16x32_bf16 v[112:115], v[180:183], v[84:87], v[112:115]\n"
      "ds_read_b128 v[180:183], v3 offset:49152\n"
      "buffer_load_dwordx4 v[196:199], v184, s[36:39], s90 offen offset:2048 sc1\n"
      "v_mfma_f32_16x16x32_bf16 v[116:119], v[240:243], v[84:87], v[116:119]\n"
      "ds_read_b128 v[240:243], v3 offset:50176\n"
      "s_waitcnt vmcnt(11)\n"
      "v_add_f32 v168, v168, v170\n"
      "v_mfma_f32_16x16x32_bf16 v[120:123], v[252:255], v[84:87], v[120:123]\n"
      "ds_read_b128 v[252:255], v3 offset:51200\n"
      "v_add_f32 v169, v169, v171\n"
      "v_mfma_f32_16x16x32_bf16 v[124:127], v[232:235], v[84:87], v[124:127]\n"
      "ds_read_b128 v[232:235], v3 offset:52224\n"
      "buffer_load_dwordx4 a[12:15], v176, s[36:39], 0 offen offset:2048 sc1\n"
      "v_mfma_f32_16x16x32_bf16 v[128:131], v[156:159], v[84:87], v[128:131]\n"
      "ds_read_b128 v[156:159], v3 offset:53248\n"
      "v_permlane32_swap_b32 v168, v169\n"
      "v_mfma_f32_16x16x32_bf16 v[132:135], v[160:163], v[84:87], v[132:135]\n"
      "ds_read_b128 v[160:163], v3 offset:54272\n"
      "v_add_f32 v168, v168, v169\n"
      "v_mfma_f32_16x16x32_bf16 v[136:139], v[212:215], v[84:87], v[136:139]\n"
      "ds_read_b128 v[212:215], v3 offset:55296\n"
      "v_mov_b32 v170, v168\n"
      "s_waitcnt lgkmcnt(7)\n"
      "v_mfma_f32_16x16x32_bf16 v[112:115], v[216:219], v[88:91], v[112:115]\n"
      "ds_read_b128 v[216:219], v3 offset:56320\n"
      "buffer_load_dwordx4 v[84:87], v184, s[36:39], s90 offen offset:3072 sc1\n"
      "v_mfma_f32_16x16x32_bf16 v[116:119], v[220:223], v[88:91], v[116:119]\n"
      "ds_read_b128 v[220:223], v3 offset:57344\n"
      "v_permlane16_swap_b32 v168, v170\n"
      "v_mfma_f32_16x16x32_bf16 v[120:123], v[224:227], v[88:91], v[120:123]\n"
      "ds_read_b128 v[224:227], v3 offset:58368\n"
      "v_add_f32 v168, v168, v170\n"
      "v_mfma_f32_16x16x32_bf16 v[124:127], v[4:7], v[88:91], v[124:127]\n"
      "ds_read_b128 v[4:7], v3 offset:59392\n"
      "buffer_load_dwordx4 a[16:19], v176, s[36:39], 0 offen offset:3072 sc1\n"
      "v_mfma_f32_16x16x32_bf16 v[128:131], v[8:11], v[88:91], v[128:131]\n"
      "ds_read_b128 v[8:11], v3 offset:60416\n"
      "v_mov_b32 v169, v168\n"
      "v_mfma_f32_16x16x32_bf16 v[132:135], v[12:15], v[88:91], v[132:135]\n"
      "ds_read_b128 v[12:15], v174 offset:0\n"
      "v_permlane32_swap_b32 v168, v169\n"
      "v_mfma_f32_16x16x32_bf16 v[136:139], v[16:19], v[88:91], v[136:139]\n"
      "ds_read_b128 v[16:19], v174 offset:1024\n"
      "v_mul_f32 v168, 0xbacccccd, v168\n"
      "s_waitcnt vmcnt(11) lgkmcnt(7)\n"
      "v_mfma_f32_16x16x32_bf16 v[112:115], v[180:183], v[92:95], v[112:115]\n"
      "ds_read_b128 v[180:183], v174 offset:2048\n"
      "buffer_load_dwordx4 v[88:91], v184, s[36:39], s87 offen offset:0 sc1\n"
      "v_mfma_f32_16x16x32_bf16 v[116:119], v[240:243], v[92:95], v[116:119]\n"
      "ds_read_b128 v[240:243], v174 offset:3072\n"
      "v_mul_f32 v169, 0x3acccccd, v169\n"
      "v_mfma_f32_16x16x32_bf16 v[120:123], v[252:255], v[92:95], v[120:123]\n"
      "ds_read_b128 v[252:255], v174 offset:4096\n"
      "v_cvt_pk_bf16_f32 v140, v168, v168\n"
      "v_mfma_f32_16x16x32_bf16 v[124:127], v[232:235], v[92:95], v[124:127]\n"
      "ds_read_b128 v[232:235], v174 offset:5120\n"
      "buffer_load_dwordx4 a[20:23], v176, s[36:39], s98 offen offset:0 sc1\n"
      "v_mfma_f32_16x16x32_bf16 v[128:131], v[156:159], v[92:95], v[128:131]\n"
      "ds_read_b128 v[156:159], v174 offset:6144\n"
      "v_fma_f32 v169, -v168, v168, v169\n"
      "v_mfma_f32_16x16x32_bf16 v[132:135], v[160:163], v[92:95], v[132:135]\n"
      "ds_read_b128 v[160:163], v174 offset:7168\n"
      "v_and_b32 v141, 0xffff0000, v140\n"
      "v_mfma_f32_16x16x32_bf16 v[136:139], v[212:215], v[92:95], v[136:139]\n"
      "ds_read_b128 v[212:215], v174 offset:8192\n"
      "v_max_f32 v169, 0, v169\n"
      "s_waitcnt vmcnt(12) lgkmcnt(7)\n"
      "v_mfma_f32_16x16x32_bf16 v[112:115], v[216:219], v[96:99], v[112:115]\n"
      "ds_read_b128 v[216:219], v174 offset:9216\n"
      "buffer_load_dwordx4 v[92:95], v184, s[36:39], s87 offen offset:1024 sc1\n"
      "v_mfma_f32_16x16x32_bf16 v[116:119], v[220:223], v[96:99], v[116:119]\n"
      "ds_read_b128 v[220:223], v174 offset:10240\n"
      "v_sub_f32 v141, v168, v141\n"
      "v_mfma_f32_16x16x32_bf16 v[120:123], v[224:227], v[96:99], v[120:123]\n"
      "ds_read_b128 v[224:227], v174 offset:11264\n"
      "v_add_f32 v169, 0x3727c5ac, v169\n"
      "v_mfma_f32_16x16x32_bf16 v[124:127], v[4:7], v[96:99], v[124:127]\n"
      "ds_read_b128 v[4:7], v174 offset:12288\n"
      "buffer_load_dwordx4 a[24:27], v176, s[36:39], s98 offen offset:1024 sc1\n"
      "v_mfma_f32_16x16x32_bf16 v[128:131], v[8:11], v[96:99], v[128:131]\n"
      "ds_read_b128 v[8:11], v174 offset:13312\n"
      "v_cvt_pk_bf16_f32 v141, v141, v141\n"
      "v_mfma_f32_16x16x32_bf16 v[132:135], v[12:15], v[96:99], v[132:135]\n"
      "ds_read_b128 v[12:15], v174 offset:14336\n"
      "v_rsq_f32 v250, v169\n"
      "v_mfma_f32_16x16x32_bf16 v[136:139], v[16:19], v[96:99], v[136:139]\n"
      "ds_read_b128 v[16:19], v174 offset:15360\n"
      "v_cndmask_b32 v140, v141, v140, s[92:93]\n"
      "s_waitcnt vmcnt(13) lgkmcnt(7)\n"
      "v_mfma_f32_16x16x32_bf16 v[112:115], v[180:183], v[100:103], v[112:115]\n"
      "ds_read_b128 v[180:183], v174 offset:16384\n"
      "buffer_load_dwordx4 v[96:99], v184, s[36:39], s87 offen offset:2048 sc1\n"
      "v_mfma_f32_16x16x32_bf16 v[116:119], v[240:243], v[100:103], v[116:119]\n"
      "ds_read_b128 v[240:243], v174 offset:17408\n"
      "v_cndmask_b32 v140, 0, v140, s[80:81]\n"
      "v_mfma_f32_16x16x32_bf16 v[120:123], v[252:255], v[100:103], v[120:123]\n"
      "ds_read_b128 v[252:255], v174 offset:18432\n"
      "v_mov_b32 v141, v140\n"
      "v_mfma_f32_16x16x32_bf16 v[124:127], v[232:235], v[100:103], v[124:127]\n"
      "ds_read_b128 v[232:235], v174 offset:19456\n"
      "buffer_load_dwordx4 a[28:31], v176, s[36:39], s98 offen offset:2048 sc1\n"
      "v_mfma_f32_16x16x32_bf16 v[128:131], v[156:159], v[100:103], v[128:131]\n"
      "ds_read_b128 v[156:159], v174 offset:20480\n"
      "v_mov_b32 v142, v140\n"
      "v_mfma_f32_16x16x32_bf16 v[132:135], v[160:163], v[100:103], v[132:135]\n"
      "ds_read_b128 v[160:163], v174 offset:21504\n"
      "v_mov_b32 v143, v140\n"
      "v_mfma_f32_16x16x32_bf16 v[136:139], v[212:215], v[100:103], v[136:139]\n"
      "ds_read_b128 v[212:215], v174 offset:22528\n"
      "v_add_f32 v188, v188, v190\n"
      "s_waitcnt vmcnt(14) lgkmcnt(7)\n"
      "v_mfma_f32_16x16x32_bf16 v[112:115], v[216:219], v[104:107], v[112:115]\n"
      "ds_read_b128 v[216:219], v174 offset:23552\n"
      "buffer_load_dwordx4 v[100:103], v184, s[36:39], s87 offen offset:3072 sc1\n"
      "v_mfma_f32_16x16x32_bf16 v[116:119], v[220:223], v[104:107], v[116:119]\n"
      "ds_read_b128 v[220:223], v174 offset:24576\n"
      "v_add_f32 v189, v189, v191\n"
      "v_mfma_f32_16x16x32_bf16 v[120:123], v[224:227], v[104:107], v[120:123]\n"
      "ds_read_b128 v[224:227], v174 offset:25600\n"
      "v_permlane32_swap_b32 v188, v189\n"
      "v_mfma_f32_16x16x32_bf16 v[124:127], v[4:7], v[104:107], v[124:127]\n"
      "ds_read_b128 v[4:7], v174 offset:26624\n"
      "buffer_load_dwordx4 a[32:35], v176, s[36:39], s98 offen offset:3072 sc1\n"
      "v_mfma_f32_16x16x32_bf16 v[128:131], v[8:11], v[104:107], v[128:131]\n"
      "ds_read_b128 v[8:11], v174 offset:27648\n"
      "v_add_f32 v188, v188, v189\n"
      "v_mfma_f32_16x16x32_bf16 v[132:135], v[12:15], v[104:107], v[132:135]\n"
      "ds_read_b128 v[12:15], v174 offset:28672\n"
      "v_mov_b32 v190, v188\n"
      "v_mfma_f32_16x16x32_bf16 v[136:139], v[16:19], v[104:107], v[136:139]\n"
      "ds_read_b128 v[16:19], v174 offset:29696\n"
      "v_permlane16_swap_b32 v188, v190\n"
      "s_waitcnt vmcnt(15) lgkmcnt(7)\n"
      "v_mfma_f32_16x16x32_bf16 v[112:115], v[180:183], v[108:111], v[112:115]\n"
      "ds_read_b128 v[180:183], v174 offset:30720\n"
      "buffer_load_dwordx4 v[104:107], v184, s[36:39], s85 offen offset:0 sc1\n"
      "v_mfma_f32_16x16x32_bf16 v[116:119], v[240:243], v[108:111], v[116:119]\n"
      "ds_read_b128 v[240:243], v174 offset:31744\n"
      "v_add_f32 v188, v188, v190\n"
      "v_mfma_f32_16x16x32_bf16 v[120:123], v[252:255], v[108:111], v[120:123]\n"
      "ds_read_b128 v[252:255], v174 offset:32768\n"
      "v_mov_b32 v189, v188\n"
      "v_mfma_f32_16x16x32_bf16 v[124:127], v[232:235], v[108:111], v[124:127]\n"
      "ds_read_b128 v[232:235], v174 offset:33792\n"
      "buffer_load_dwordx4 a[36:39], v176, s[36:39], s90 offen offset:0 sc1\n"
      "v_mfma_f32_16x16x32_bf16 v[128:131], v[156:159], v[108:111], v[128:131]\n"
      "ds_read_b128 v[156:159], v174 offset:34816\n"
      "v_permlane32_swap_b32 v188, v189\n"
      "v_mfma_f32_16x16x32_bf16 v[132:135], v[160:163], v[108:111], v[132:135]\n"
      "ds_read_b128 v[160:163], v174 offset:35840\n"
      "v_mul_f32 v188, 0xbacccccd, v188\n"
      "v_mfma_f32_16x16x32_bf16 v[136:139], v[212:215], v[108:111], v[136:139]\n"
      "ds_read_b128 v[212:215], v174 offset:36864\n"
      "v_mul_f32 v189, 0x3acccccd, v189\n"
      "s_waitcnt vmcnt(15) lgkmcnt(7)\n"
      "v_mfma_f32_16x16x32_bf16 v[112:115], v[216:219], v[192:195], v[112:115]\n"
      "ds_read_b128 v[216:219], v174 offset:37888\n"
      "buffer_load_dwordx4 v[108:111], v184, s[36:39], s85 offen offset:1024 sc1\n"
      "v_mfma_f32_16x16x32_bf16 v[116:119], v[220:223], v[192:195], v[116:119]\n"
      "ds_read_b128 v[220:223], v174 offset:38912\n"
      "v_cvt_pk_bf16_f32 v144, v188, v188\n"
      "v_mfma_f32_16x16x32_bf16 v[120:123], v[224:227], v[192:195], v[120:123]\n"
      "ds_read_b128 v[224:227], v174 offset:39936\n"
      "v_fma_f32 v189, -v188, v188, v189\n"
      "v_mfma_f32_16x16x32_bf16 v[124:127], v[4:7], v[192:195], v[124:127]\n"
      "ds_read_b128 v[4:7], v174 offset:40960\n"
      "buffer_load_dwordx4 a[40:43], v176, s[36:39], s90 offen offset:1024 sc1\n"
      "v_mfma_f32_16x16x32_bf16 v[128:131], v[8:11], v[192:195], v[128:131]\n"
      "ds_read_b128 v[8:11], v174 offset:41984\n"
      "v_and_b32 v145, 0xffff0000, v144\n"
      "v_mfma_f32_16x16x32_bf16 v[132:135], v[12:15], v[192:195], v[132:135]\n"
      "ds_read_b128 v[12:15], v174 offset:43008\n"
      "v_max_f32 v189, 0, v189\n"
      "v_mfma_f32_16x16x32_bf16 v[136:139], v[16:19], v[192:195], v[136:139]\n"
      "ds_read_b128 v[16:19], v174 offset:44032\n"
      "v_sub_f32 v145, v188, v145\n"
      "s_waitcnt vmcnt(15) lgkmcnt(7)\n"
      "v_mfma_f32_16x16x32_bf16 v[112:115], v[180:183], v[196:199], v[112:115]\n"
      "ds_read_b128 v[180:183], v174 offset:45056\n"
      "buffer_load_dwordx4 v[192:195], v184, s[36:39], s85 offen offset:2048 sc1\n"
      "v_mfma_f32_16x16x32_bf16 v[116:119], v[240:243], v[196:199], v[116:119]\n"
      "ds_read_b128 v[240:243], v174 offset:46080\n"
      "v_add_f32 v189, 0x3727c5ac, v189\n"
      "v_mfma_f32_16x16x32_bf16 v[120:123], v[252:255], v[196:199], v[120:123]\n"
      "ds_read_b128 v[252:255], v174 offset:47104\n"
      "v_cvt_pk_bf16_f32 v145, v145, v145\n"
      "v_mfma_f32_16x16x32_bf16 v[124:127], v[232:235], v[196:199], v[124:127]\n"
      "ds_read_b128 v[232:235], v174 offset:48128\n"
      "buffer_load_dwordx4 a[44:47], v176, s[36:39], s90 offen offset:2048 sc1\n"
      "v_mfma_f32_16x16x32_bf16 v[128:131], v[156:159], v[196:199], v[128:131]\n"
      "ds_read_b128 v[156:159], v174 offset:49152\n"
      "v_rsq_f32 v246, v189\n"
      "v_mfma_f32_16x16x32_bf16 v[132:135], v[160:163], v[196:199], v[132:135]\n"
      "ds_read_b128 v[160:163], v174 offset:50176\n"
      "v_cndmask_b32 v144, v145, v144, s[92:93]\n"
      "v_mfma_f32_16x16x32_bf16 v[136:139], v[212:215], v[196:199], v[136:139]\n"
      "ds_read_b128 v[212:215], v174 offset:51200\n"
      "v_cndmask_b32 v144, 0, v144, s[80:81]\n"
      "s_waitcnt vmcnt(15) lgkmcnt(7)\n"
      "v_mfma_f32_16x16x32_bf16 v[112:115], v[216:219], v[84:87], v[112:115]\n"
      "ds_read_b128 v[216:219], v174 offset:52224\n"
      "buffer_load_dwordx4 v[196:199], v184, s[36:39], s85 offen offset:3072 sc1\n"
      "v_mfma_f32_16x16x32_bf16 v[116:119], v[220:223], v[84:87], v[116:119]\n"
      "ds_read_b128 v[220:223], v174 offset:53248\n"
      "v_mov_b32 v145, v144\n"
      "v_mfma_f32_16x16x32_bf16 v[120:123], v[224:227], v[84:87], v[120:123]\n"
      "ds_read_b128 v[224:227], v174 offset:54272\n"
      "v_mov_b32 v146, v144\n"
      "v_mfma_f32_16x16x32_bf16 v[124:127], v[4:7], v[84:87], v[124:127]\n"
      "ds_read_b128 v[4:7], v174 offset:55296\n"
      "buffer_load_dwordx4 a[48:51], v176, s[36:39], s90 offen offset:3072 sc1\n"
      "v_mfma_f32_16x16x32_bf16 v[128:131], v[8:11], v[84:87], v[128:131]\n"
      "ds_read_b128 v[8:11], v174 offset:56320\n"
      "v_mov_b32 v147, v144\n"
      "v_mfma_f32_16x16x32_bf16 v[132:135], v[12:15], v[84:87], v[132:135]\n"
      "ds_read_b128 v[12:15], v174 offset:57344\n"
      "v_mfma_f32_16x16x32_bf16 v[136:139], v[16:19], v[84:87], v[136:139]\n"
      "ds_read_b128 v[16:19], v174 offset:58368\n"
      "s_waitcnt vmcnt(15) lgkmcnt(7)\n"
      "v_mfma_f32_16x16x32_bf16 v[112:115], v[180:183], v[88:91], v[112:115]\n"
      "ds_read_b128 v[180:183], v174 offset:59392\n"
      "buffer_load_dwordx4 a[52:55], v176, s[36:39], s87 offen offset:0 sc1\n"
      "v_mfma_f32_16x16x32_bf16 v[116:119], v[240:243], v[88:91], v[116:119]\n"
      "ds_read_b128 v[240:243], v174 offset:60416\n"
      "v_mfma_f32_16x16x32_bf16 v[120:123], v[252:255], v[88:91], v[120:123]\n"
      "ds_read_b128 v[252:255], v178 offset:0\n"
      "v_mfma_f32_16x16x32_bf16 v[124:127], v[232:235], v[88:91], v[124:127]\n"
      "ds_read_b128 v[232:235], v178 offset:1024\n"
      "buffer_load_dwordx4 a[56:59], v176, s[36:39], s87 offen offset:1024 sc1\n"
      "v_mfma_f32_16x16x32_bf16 v[128:131], v[156:159], v[88:91], v[128:131]\n"
      "ds_read_b128 v[156:159], v178 offset:2048\n"
      "v_mfma_f32_16x16x32_bf16 v[132:135], v[160:163], v[88:91], v[132:135]\n"
      "ds_read_b128 v[160:163], v178 offset:3072\n"
      "v_mfma_f32_16x16x32_bf16 v[136:139], v[212:215], v[88:91], v[136:139]\n"
      "ds_read_b128 v[212:215], v178 offset:4096\n"
      "s_waitcnt vmcnt(15) lgkmcnt(7)\n"
      "v_mfma_f32_16x16x32_bf16 v[112:115], v[216:219], v[92:95], v[112:115]\n"
      "ds_read_b128 v[216:219], v178 offset:5120\n"
      "buffer_load_dwordx4 a[60:63], v176, s[36:39], s87 offen offset:2048 sc1\n"
      "v_mfma_f32_16x16x32_bf16 v[116:119], v[220:223], v[92:95], v[116:119]\n"
      "ds_read_b128 v[220:223], v178 offset:6144\n"
      "v_mfma_f32_16x16x32_bf16 v[120:123], v[224:227], v[92:95], v[120:123]\n"
      "ds_read_b128 v[224:227], v178 offset:7168\n"
      "v_mfma_f32_16x16x32_bf16 v[124:127], v[4:7], v[92:95], v[124:127]\n"
      "ds_read_b128 v[4:7], v178 offset:8192\n"
      "buffer_load_dwordx4 a[64:67], v176, s[36:39], s87 offen offset:3072 sc1\n"
      "v_mfma_f32_16x16x32_bf16 v[128:131], v[8:11], v[92:95], v[128:131]\n"
      "ds_read_b128 v[8:11], v178 offset:9216\n"
      "v_mfma_f32_16x16x32_bf16 v[132:135], v[12:15], v[92:95], v[132:135]\n"
      "ds_read_b128 v[12:15], v178 offset:10240\n"
      "v_mfma_f32_16x16x32_bf16 v[136:139], v[16:19], v[92:95], v[136:139]\n"
      "ds_read_b128 v[16:19], v178 offset:11264\n"
      "s_waitcnt vmcnt(15) lgkmcnt(7)\n"
      "v_mfma_f32_16x16x32_bf16 v[112:115], v[180:183], v[96:99], v[112:115]\n"
      "ds_read_b128 v[180:183], v178 offset:12288\n"
      "buffer_load_dwordx4 a[68:71], v176, s[36:39], s85 offen offset:0 sc1\n"
      "v_mfma_f32_16x16x32_bf16 v[116:119], v[240:243], v[96:99], v[116:119]\n"
      "ds_read_b128 v[240:243], v178 offset:13312\n"
      "v_mfma_f32_16x16x32_bf16 v[120:123], v[252:255], v[96:99], v[120:123]\n"
      "ds_read_b128 v[252:255], v178 offset:14336\n"
      "v_mfma_f32_16x16x32_bf16 v[124:127], v[232:235], v[96:99], v[124:127]\n"
      "ds_read_b128 v[232:235], v178 offset:15360\n"
      "buffer_load_dwordx4 a[72:75], v176, s[36:39], s85 offen offset:1024 sc1\n"
      "v_mfma_f32_16x16x32_bf16 v[128:131], v[156:159], v[96:99], v[128:131]\n"
      "ds_read_b128 v[156:159], v178 offset:16384\n"
      "v_mfma_f32_16x16x32_bf16 v[132:135], v[160:163], v[96:99], v[132:135]\n"
      "ds_read_b128 v[160:163], v178 offset:17408\n"
      "v_mfma_f32_16x16x32_bf16 v[136:139], v[212:215], v[96:99], v[136:139]\n"
      "ds_read_b128 v[212:215], v178 offset:18432\n"
      "s_waitcnt vmcnt(15) lgkmcnt(7)\n"
      "v_mfma_f32_16x16x32_bf16 v[112:115], v[216:219], v[100:103], v[112:115]\n"
      "ds_read_b128 v[216:219], v178 offset:19456\n"
      "buffer_load_dwordx4 a[76:79], v176, s[36:39], s85 offen offset:2048 sc1\n"
      "v_mfma_f32_16x16x32_bf16 v[116:119], v[220:223], v[100:103], v[116:119]\n"
      "ds_read_b128 v[220:223], v178 offset:20480\n"
      "v_mfma_f32_16x16x32_bf16 v[120:123], v[224:227], v[100:103], v[120:123]\n"
      "ds_read_b128 v[224:227], v178 offset:21504\n"
      "v_mfma_f32_16x16x32_bf16 v[124:127], v[4:7], v[100:103], v[124:127]\n"
      "ds_read_b128 v[4:7], v178 offset:22528\n"
      "buffer_load_dwordx4 a[80:83], v176, s[36:39], s85 offen offset:3072 sc1\n"
      "v_mfma_f32_16x16x32_bf16 v[128:131], v[8:11], v[100:103], v[128:131]\n"
      "ds_read_b128 v[8:11], v178 offset:23552\n"
      "v_mfma_f32_16x16x32_bf16 v[132:135], v[12:15], v[100:103], v[132:135]\n"
      "ds_read_b128 v[12:15], v178 offset:24576\n"
      "s_add_u32 s96, s97, 40960\n"
      "buffer_load_dwordx4 a[136:139], v239, s[36:39], s96 offen offset:0 sc1\n"
      "v_mfma_f32_16x16x32_bf16 v[136:139], v[16:19], v[100:103], v[136:139]\n"
      "ds_read_b128 v[16:19], v178 offset:25600\n"
      "s_waitcnt vmcnt(16) lgkmcnt(7)\n"
      "v_mfma_f32_16x16x32_bf16 v[112:115], v[180:183], v[104:107], v[112:115]\n"
      "ds_read_b128 v[180:183], v178 offset:26624\n"
      "v_mfma_f32_16x16x32_bf16 v[116:119], v[240:243], v[104:107], v[116:119]\n"
      "ds_read_b128 v[240:243], v178 offset:27648\n"
      "v_mfma_f32_16x16x32_bf16 v[120:123], v[252:255], v[104:107], v[120:123]\n"
      "ds_read_b128 v[252:255], v178 offset:28672\n"
      "v_mfma_f32_16x16x32_bf16 v[124:127], v[232:235], v[104:107], v[124:127]\n"
      "ds_read_b128 v[232:235], v178 offset:29696\n"
      "buffer_load_dwordx4 a[140:143], v239, s[36:39], s96 offen offset:1024 sc1\n"
      "v_mfma_f32_16x16x32_bf16 v[128:131], v[156:159], v[104:107], v[128:131]\n"
      "ds_read_b128 v[156:159], v178 offset:30720\n"
      "v_mfma_f32_16x16x32_bf16 v[132:135], v[160:163], v[104:107], v[132:135]\n"
      "ds_read_b128 v[160:163], v178 offset:31744\n"
      "s_mov_b32 s96, 45056\n"
      "buffer_load_dwordx4 a[144:147], v239, s[36:39], s96 offen offset:0 sc1\n"
      "v_mfma_f32_16x16x32_bf16 v[136:139], v[212:215], v[104:107], v[136:139]\n"
      "ds_read_b128 v[212:215], v178 offset:32768\n"
      "s_waitcnt vmcnt(16) lgkmcnt(7)\n"
      "v_mfma_f32_16x16x32_bf16 v[112:115], v[216:219], v[108:111], v[112:115]\n"
      "ds_read_b128 v[216:219], v178 offset:33792\n"
      "v_mfma_f32_16x16x32_bf16 v[116:119], v[220:223], v[108:111], v[116:119]\n"
      "ds_read_b128 v[220:223], v178 offset:34816\n"
      "v_mfma_f32_16x16x32_bf16 v[120:123], v[224:227], v[108:111], v[120:123]\n"
      "ds_read_b128 v[224:227], v178 offset:35840\n"
      "v_mfma_f32_16x16x32_bf16 v[124:127], v[4:7], v[108:111], v[124:127]\n"
      "ds_read_b128 v[4:7], v178 offset:36864\n"
      "s_add_u32 s96, s83, 40960\n"
      "buffer_load_dwordx4 v[20:23], v239, s[36:39], s96 offen offset:0 sc1\n"
      "v_mfma_f32_16x16x32_bf16 v[128:131], v[8:11], v[108:111], v[128:131]\n"
      "ds_read_b128 v[8:11], v178 offset:37888\n"
      "v_mfma_f32_16x16x32_bf16 v[132:135], v[12:15], v[108:111], v[132:135]\n"
      "ds_read_b128 v[12:15], v178 offset:38912\n"
      "buffer_load_dwordx4 v[24:27], v239, s[36:39], s96 offen offset:1024 sc1\n"
      "v_mfma_f32_16x16x32_bf16 v[136:139], v[16:19], v[108:111], v[136:139]\n"
      "ds_read_b128 v[16:19], v178 offset:39936\n"
      "s_waitcnt vmcnt(16) lgkmcnt(7)\n"
      "v_mfma_f32_16x16x32_bf16 v[112:115], v[180:183], v[192:195], v[112:115]\n"
      "ds_read_b128 v[180:183], v187 offset:0\n"
      "v_mfma_f32_16x16x32_bf16 v[116:119], v[240:243], v[192:195], v[116:119]\n"
      "ds_read_b128 v[240:243], v187 offset:256\n"
      "v_mfma_f32_16x16x32_bf16 v[120:123], v[252:255], v[192:195], v[120:123]\n"
      "ds_read_b128 v[252:255], v187 offset:512\n"
      "v_mfma_f32_16x16x32_bf16 v[124:127], v[232:235], v[192:195], v[124:127]\n"
      "ds_read_b128 v[232:235], v187 offset:768\n"
      "s_add_u32 s96, s97, 46080\n"
      "buffer_load_dwordx4 v[28:31], v239, s[36:39], s96 offen offset:0 sc1\n"
      "v_mfma_f32_16x16x32_bf16 v[128:131], v[156:159], v[192:195], v[128:131]\n"
      "ds_read_b128 v[156:159], v187 offset:1024\n"
      "v_mfma_f32_16x16x32_bf16 v[132:135], v[160:163], v[192:195], v[132:135]\n"
      "ds_read_b128 v[160:163], v187 offset:1280\n"
      "buffer_load_dwordx4 v[32:35], v239, s[36:39], s96 offen offset:1024 sc1\n"
      "v_mfma_f32_16x16x32_bf16 v[136:139], v[212:215], v[192:195], v[136:139]\n"
      "ds_read_b128 v[212:215], v187 offset:1536\n"
      "s_waitcnt vmcnt(16) lgkmcnt(7)\n"
      "v_mfma_f32_16x16x32_bf16 v[112:115], v[216:219], v[196:199], v[112:115]\n"
      "ds_read_b128 v[216:219], v3 offset:20480\n"
      "v_mfma_f32_16x16x32_bf16 v[116:119], v[220:223], v[196:199], v[116:119]\n"
      "ds_read_b128 v[220:223], v3 offset:21504\n"
      "v_mfma_f32_16x16x32_bf16 v[120:123], v[224:227], v[196:199], v[120:123]\n"
      "ds_read_b128 v[224:227], v3 offset:22528\n"
      "v_mfma_f32_16x16x32_bf16 v[124:127], v[4:7], v[196:199], v[124:127]\n"
      "ds_read_b128 v[4:7], v3 offset:23552\n"
      "s_mov_b32 s96, 50176\n"
      "buffer_load_dwordx4 v[36:39], v239, s[36:39], s96 offen offset:0 sc1\n"
      "v_mfma_f32_16x16x32_bf16 v[128:131], v[8:11], v[196:199], v[128:131]\n"
      "ds_read_b128 v[8:11], v3 offset:24576\n"
      "v_mfma_f32_16x16x32_bf16 v[132:135], v[12:15], v[196:199], v[132:135]\n"
      "ds_read_b128 v[12:15], v3 offset:25600\n"
      "s_add_u32 s96, s83, 46080\n"
      "buffer_load_dwordx4 v[40:43], v239, s[36:39], s96 offen offset:0 sc1\n"
      "v_mfma_f32_16x16x32_bf16 v[136:139], v[16:19], v[196:199], v[136:139]\n"
      "ds_read_b128 v[16:19], v3 offset:26624\n"
      "s_waitcnt lgkmcnt(7)\n"
      "v_mfma_f32_16x16x32_bf16 v[112:115], v[180:183], v[140:143], v[112:115]\n"
      "ds_read_b128 v[180:183], v3 offset:27648\n"
      "v_mfma_f32_16x16x32_bf16 v[116:119], v[240:243], v[140:143], v[116:119]\n"
      "ds_read_b128 v[240:243], v3 offset:28672\n"
      "v_mfma_f32_16x16x32_bf16 v[120:123], v[252:255], v[140:143], v[120:123]\n"
      "ds_read_b128 v[252:255], v3 offset:29696\n"
      "v_mfma_f32_16x16x32_bf16 v[124:127], v[232:235], v[140:143], v[124:127]\n"
      "ds_read_b128 v[232:235], v3 offset:30720\n"
      "v_mfma_f32_16x16x32_bf16 v[128:131], v[156:159], v[140:143], v[128:131]\n"
      "ds_read_b128 v[156:159], v3 offset:31744\n"
      "v_mfma_f32_16x16x32_bf16 v[132:135], v[160:163], v[140:143], v[132:135]\n"
      "ds_read_b128 v[160:163], v3 offset:32768\n"
      "v_mfma_f32_16x16x32_bf16 v[136:139], v[212:215], v[140:143], v[136:139]\n"
      "ds_read_b128 v[212:215], v3 offset:33792\n"
      "s_waitcnt lgkmcnt(7)\n"
      "v_mfma_f32_16x16x32_bf16 v[204:207], v[216:219], a[4:7], 0\n"
      "ds_read_b128 v[216:219], v3 offset:34816\n"
      "v_mfma_f32_16x16x32_bf16 v[208:211], v[220:223], a[4:7], 0\n"
      "ds_read_b128 v[220:223], v3 offset:35840\n"
      "v_mfma_f32_16x16x32_bf16 v[44:47], v[224:227], a[4:7], 0\n"
      "ds_read_b128 v[224:227], v3 offset:36864\n"
      "v_mul_f32 v112, v112, v250\n"
      "v_mul_f32 v113, v113, v250\n"
      "v_mul_f32 v114, v114, v250\n"
      "v_mfma_f32_16x16x32_bf16 v[48:51], v[4:7], a[4:7], 0\n"
      "ds_read_b128 v[4:7], v3 offset:37888\n"
      "buffer_load_dwordx4 a[0:3], v239, s[36:39], s96 offen offset:1024 sc1\n"
      "v_mfma_f32_16x16x32_bf16 v[52:55], v[8:11], a[4:7], 0\n"
      "ds_read_b128 v[8:11], v3 offset:38912\n"
      "v_mul_f32 v115, v115, v250\n"
      "v_cvt_pk_bf16_f32 v168, v112, v113\n"
      "v_cvt_pk_bf16_f32 v169, v114, v115\n"
      "v_mfma_f32_16x16x32_bf16 v[76:79], v[12:15], a[4:7], 0\n"
      "ds_read_b128 v[12:15], v3 offset:39936\n"
      "v_pk_max_i16 v168, v168, 0\n"
      "v_pk_max_i16 v169, v169, 0\n"
      "v_mul_f32 v116, v116, v250\n"
      "v_mfma_f32_16x16x32_bf16 v[80:83], v[16:19], a[4:7], 0\n"
      "ds_read_b128 v[16:19], v3 offset:40960\n"
      "v_mul_f32 v117, v117, v250\n"
      "v_mul_f32 v118, v118, v250\n"
      "v_mul_f32 v119, v119, v250\n"
      "s_waitcnt lgkmcnt(7)\n"
      "v_mfma_f32_16x16x32_bf16 v[204:207], v[180:183], a[8:11], v[204:207]\n"
      "ds_read_b128 v[180:183], v3 offset:41984\n"
      "v_cvt_pk_bf16_f32 v170, v116, v117\n"
      "v_cvt_pk_bf16_f32 v171, v118, v119\n"
      "v_pk_max_i16 v170, v170, 0\n"
      "v_mfma_f32_16x16x32_bf16 v[208:211], v[240:243], a[8:11], v[208:211]\n"
      "ds_read_b128 v[240:243], v3 offset:43008\n"
      "v_pk_max_i16 v171, v171, 0\n"
      "buffer_store_dwordx4 v[168:171], v152, s[36:39], 0 offen offset:0 sc1\n"
      "v_mul_f32 v120, v120, v250\n"
      "v_mfma_f32_16x16x32_bf16 v[44:47], v[252:255], a[8:11], v[44:47]\n"
      "ds_read_b128 v[252:255], v3 offset:44032\n"
      "v_mul_f32 v121, v121, v250\n"
      "v_mul_f32 v122, v122, v250\n"
      "v_mul_f32 v123, v123, v250\n"
      "v_mfma_f32_16x16x32_bf16 v[48:51], v[232:235], a[8:11], v[48:51]\n"
      "ds_read_b128 v[232:235], v3 offset:45056\n"
      "s_add_u32 s96, s97, 51200\n"
      "buffer_load_dwordx4 a[4:7], v239, s[36:39], s96 offen offset:0 sc1\n"
      "v_mfma_f32_16x16x32_bf16 v[52:55], v[156:159], a[8:11], v[52:55]\n"
      "ds_read_b128 v[156:159], v3 offset:46080\n"
      "v_cvt_pk_bf16_f32 v188, v120, v121\n"
      "v_cvt_pk_bf16_f32 v189, v122, v123\n"
      "v_pk_max_i16 v188, v188, 0\n"
      "v_mfma_f32_16x16x32_bf16 v[76:79], v[160:163], a[8:11], v[76:79]\n"
      "ds_read_b128 v[160:163], v3 offset:47104\n"
      "v_pk_max_i16 v189, v189, 0\n"
      "v_mul_f32 v124, v124, v250\n"
      "v_mul_f32 v125, v125, v250\n"
      "v_mfma_f32_16x16x32_bf16 v[80:83], v[212:215], a[8:11], v[80:83]\n"
      "ds_read_b128 v[212:215], v3 offset:48128\n"
      "v_mul_f32 v126, v126, v250\n"
      "v_mul_f32 v127, v127, v250\n"
      "v_cvt_pk_bf16_f32 v190, v124, v125\n"
      "s_waitcnt lgkmcnt(7)\n"
      "v_mfma_f32_16x16x32_bf16 v[204:207], v[216:219], a[12:15], v[204:207]\n"
      "ds_read_b128 v[216:219], v3 offset:49152\n"
      "v_cvt_pk_bf16_f32 v191, v126, v127\n"
      "v_pk_max_i16 v190, v190, 0\n"
      "v_pk_max_i16 v191, v191, 0\n"
      "v_mfma_f32_16x16x32_bf16 v[208:211], v[220:223], a[12:15], v[208:211]\n"
      "ds_read_b128 v[220:223], v3 offset:50176\n"
      "buffer_store_dwordx4 v[188:191], v152, s[36:39], 0 offen offset:1024 sc1\n"
      "v_mul_f32 v128, v128, v250\n"
      "v_mul_f32 v129, v129, v250\n"
      "v_mfma_f32_16x16x32_bf16 v[44:47], v[224:227], a[12:15], v[44:47]\n"
      "ds_read_b128 v[224:227], v3 offset:51200\n"
      "v_mul_f32 v130, v130, v250\n"
      "v_mul_f32 v131, v131, v250\n"
      "v_cvt_pk_bf16_f32 v168, v128, v129\n"
      "v_mfma_f32_16x16x32_bf16 v[48:51], v[4:7], a[12:15], v[48:51]\n"
      "ds_read_b128 v[4:7], v3 offset:52224\n"
      "buffer_load_dwordx4 a[8:11], v239, s[36:39], s96 offen offset:1024 sc1\n"
      "v_mfma_f32_16x16x32_bf16 v[52:55], v[8:11], a[12:15], v[52:55]\n"
      "ds_read_b128 v[8:11], v3 offset:53248\n"
      "v_cvt_pk_bf16_f32 v169, v130, v131\n"
      "v_pk_max_i16 v168, v168, 0\n"
      "v_pk_max_i16 v169, v169, 0\n"
      "v_mfma_f32_16x16x32_bf16 v[76:79], v[12:15], a[12:15], v[76:79]\n"
      "ds_read_b128 v[12:15], v3 offset:54272\n"
      "v_mul_f32 v132, v132, v250\n"
      "v_mul_f32 v133, v133, v250\n"
      "v_mul_f32 v134, v134, v250\n"
      "v_mfma_f32_16x16x32_bf16 v[80:83], v[16:19], a[12:15], v[80:83]\n"
      "ds_read_b128 v[16:19], v3 offset:55296\n"
      "v_mul_f32 v135, v135, v250\n"
      "v_cvt_pk_bf16_f32 v170, v132, v133\n"
      "v_cvt_pk_bf16_f32 v171, v134, v135\n"
      "s_waitcnt lgkmcnt(7)\n"
      "v_mfma_f32_16x16x32_bf16 v[204:207], v[180:183], a[16:19], v[204:207]\n"
      "ds_read_b128 v[180:183], v3 offset:56320\n"
      "v_pk_max_i16 v170, v170, 0\n"
      "v_pk_max_i16 v171, v171, 0\n"
      "buffer_store_dwordx4 v[168:171], v152, s[36:39], 0 offen offset:2048 sc1\n"
      "v_mfma_f32_16x16x32_bf16 v[208:211], v[240:243], a[16:19], v[208:211]\n"
      "ds_read_b128 v[240:243], v3 offset:57344\n"
      "v_mul_f32 v136, v136, v250\n"
      "v_mul_f32 v137, v137, v250\n"
      "v_mul_f32 v138, v138, v250\n"
      "v_mfma_f32_16x16x32_bf16 v[44:47], v[252:255], a[16:19], v[44:47]\n"
      "ds_read_b128 v[252:255], v3 offset:58368\n"
      "v_mul_f32 v139, v139, v250\n"
      "v_cvt_pk_bf16_f32 v188, v136, v137\n"
      "v_cvt_pk_bf16_f32 v189, v138, v139\n"
      "v_mfma_f32_16x16x32_bf16 v[48:51], v[232:235], a[16:19], v[48:51]\n"
      "ds_read_b128 v[232:235], v3 offset:59392\n"
      "s_mov_b32 s96, 55296\n"
      "buffer_load_dwordx4 a[12:15], v239, s[36:39], s96 offen offset:0 sc1\n"
      "v_mfma_f32_16x16x32_bf16 v[52:55], v[156:159], a[16:19], v[52:55]\n"
      "ds_read_b128 v[156:159], v3 offset:60416\n"
      "v_pk_max_i16 v188, v188, 0\n"
      "v_pk_max_i16 v189, v189, 0\n"
      "s_nop 0\n"
      "v_permlane32_swap_b32 v188, v190\n"
      "v_mfma_f32_16x16x32_bf16 v[76:79], v[160:163], a[16:19], v[76:79]\n"
      "ds_read_b128 v[160:163], v174 offset:0\n"
      "v_permlane32_swap_b32 v189, v191\n"
      "buffer_store_dwordx4 v[188:191], v249, s[36:39], 0 offen sc1\n"
      "v_mfma_f32_16x16x32_bf16 v[80:83], v[212:215], a[16:19], v[80:83]\n"
      "ds_read_b128 v[212:215], v174 offset:1024\n"
      "s_waitcnt lgkmcnt(7)\n"
      "v_mfma_f32_16x16x32_bf16 v[204:207], v[216:219], a[20:23], v[204:207]\n"
      "ds_read_b128 v[216:219], v174 offset:2048\n"
      "v_mfma_f32_16x16x32_bf16 v[208:211], v[220:223], a[20:23], v[208:211]\n"
      "ds_read_b128 v[220:223], v174 offset:3072\n"
      "v_mfma_f32_16x16x32_bf16 v[44:47], v[224:227], a[20:23], v[44:47]\n"
      "ds_read_b128 v[224:227], v174 offset:4096\n"
      "v_mfma_f32_16x16x32_bf16 v[48:51], v[4:7], a[20:23], v[48:51]\n"
      "ds_read_b128 v[4:7], v174 offset:5120\n"
      "s_add_u32 s96, s83, 51200\n"
      "buffer_load_dwordx4 a[16:19], v239, s[36:39], s96 offen offset:0 sc1\n"
      "v_mfma_f32_16x16x32_bf16 v[52:55], v[8:11], a[20:23], v[52:55]\n"
      "ds_read_b128 v[8:11], v174 offset:6144\n"
      "v_mfma_f32_16x16x32_bf16 v[76:79], v[12:15], a[20:23], v[76:79]\n"
      "ds_read_b128 v[12:15], v174 offset:7168\n"
      "v_mfma_f32_16x16x32_bf16 v[80:83], v[16:19], a[20:23], v[80:83]\n"
      "ds_read_b128 v[16:19], v174 offset:8192\n"
      "s_waitcnt lgkmcnt(7)\n"
      "v_mfma_f32_16x16x32_bf16 v[204:207], v[180:183], a[24:27], v[204:207]\n"
      "ds_read_b128 v[180:183], v174 offset:9216\n"
      "v_mfma_f32_16x16x32_bf16 v[208:211], v[240:243], a[24:27], v[208:211]\n"
      "ds_read_b128 v[240:243], v174 offset:10240\n"
      "v_mfma_f32_16x16x32_bf16 v[44:47], v[252:255], a[24:27], v[44:47]\n"
      "ds_read_b128 v[252:255], v174 offset:11264\n"
      "v_mfma_f32_16x16x32_bf16 v[48:51], v[232:235], a[24:27], v[48:51]\n"
      "ds_read_b128 v[232:235], v174 offset:12288\n"
      "buffer_load_dwordx4 a[20:23], v239, s[36:39], s96 offen offset:1024 sc1\n"
      "v_mfma_f32_16x16x32_bf16 v[52:55], v[156:159], a[24:27], v[52:55]\n"
      "ds_read_b128 v[156:159], v174 offset:13312\n"
      "v_mfma_f32_16x16x32_bf16 v[76:79], v[160:163], a[24:27], v[76:79]\n"
      "ds_read_b128 v[160:163], v174 offset:14336\n"
      "v_mfma_f32_16x16x32_bf16 v[80:83], v[212:215], a[24:27], v[80:83]\n"
      "ds_read_b128 v[212:215], v174 offset:15360\n"
      "s_waitcnt lgkmcnt(7)\n"
      "v_mfma_f32_16x16x32_bf16 v[204:207], v[216:219], a[28:31], v[204:207]\n"
      "ds_read_b128 v[216:219], v174 offset:16384\n"
      "v_mfma_f32_16x16x32_bf16 v[208:211], v[220:223], a[28:31], v[208:211]\n"
      "ds_read_b128 v[220:223], v174 offset:17408\n"
      "v_mfma_f32_16x16x32_bf16 v[44:47], v[224:227], a[28:31], v[44:47]\n"
      "ds_read_b128 v[224:227], v174 offset:18432\n"
      "v_mfma_f32_16x16x32_bf16 v[48:51], v[4:7], a[28:31], v[48:51]\n"
      "ds_read_b128 v[4:7], v174 offset:19456\n"
      "s_add_u32 s96, s97, 56320\n"
      "buffer_load_dwordx4 a[24:27], v239, s[36:39], s96 offen offset:0 sc1\n"
      "v_mfma_f32_16x16x32_bf16 v[52:55], v[8:11], a[28:31], v[52:55]\n"
      "ds_read_b128 v[8:11], v174 offset:20480\n"
      "v_mfma_f32_16x16x32_bf16 v[76:79], v[12:15], a[28:31], v[76:79]\n"
      "ds_read_b128 v[12:15], v174 offset:21504\n"
      "v_mfma_f32_16x16x32_bf16 v[80:83], v[16:19], a[28:31], v[80:83]\n"
      "ds_read_b128 v[16:19], v174 offset:22528\n"
      "s_waitcnt lgkmcnt(7)\n"
      "v_mfma_f32_16x16x32_bf16 v[204:207], v[180:183], a[32:35], v[204:207]\n"
      "ds_read_b128 v[180:183], v174 offset:23552\n"
      "v_mfma_f32_16x16x32_bf16 v[208:211], v[240:243], a[32:35], v[208:211]\n"
      "ds_read_b128 v[240:243], v174 offset:24576\n"
      "v_mfma_f32_16x16x32_bf16 v[44:47], v[252:255], a[32:35], v[44:47]\n"
      "ds_read_b128 v[252:255], v174 offset:25600\n"
      "v_mfma_f32_16x16x32_bf16 v[48:51], v[232:235], a[32:35], v[48:51]\n"
      "ds_read_b128 v[232:235], v174 offset:26624\n"
      "buffer_load_dwordx4 a[28:31], v239, s[36:39], s96 offen offset:1024 sc1\n"
      "v_mfma_f32_16x16x32_bf16 v[52:55], v[156:159], a[32:35], v[52:55]\n"
      "ds_read_b128 v[156:159], v174 offset:27648\n"
      "v_mfma_f32_16x16x32_bf16 v[76:79], v[160:163], a[32:35], v[76:79]\n"
      "ds_read_b128 v[160:163], v174 offset:28672\n"
      "v_mfma_f32_16x16x32_bf16 v[80:83], v[212:215], a[32:35], v[80:83]\n"
      "ds_read_b128 v[212:215], v174 offset:29696\n"
      "s_waitcnt lgkmcnt(7)\n"
      "v_mfma_f32_16x16x32_bf16 v[204:207], v[216:219], a[36:39], v[204:207]\n"
      "ds_read_b128 v[216:219], v174 offset:30720\n"
      "v_mfma_f32_16x16x32_bf16 v[208:211], v[220:223], a[36:39], v[208:211]\n"
      "ds_read_b128 v[220:223], v174 offset:31744\n"
      "v_mfma_f32_16x16x32_bf16 v[44:47], v[224:227], a[36:39], v[44:47]\n"
      "ds_read_b128 v[224:227], v174 offset:32768\n"
      "v_mfma_f32_16x16x32_bf16 v[48:51], v[4:7], a[36:39], v[48:51]\n"
      "ds_read_b128 v[4:7], v174 offset:33792\n"
      "s_mov_b32 s96, 60416\n"
      "buffer_load_dwordx4 a[32:35], v239, s[36:39], s96 offen offset:0 sc1\n"
      "v_mfma_f32_16x16x32_bf16 v[52:55], v[8:11], a[36:39], v[52:55]\n"
      "ds_read_b128 v[8:11], v174 offset:34816\n"
      "v_mfma_f32_16x16x32_bf16 v[76:79], v[12:15], a[36:39], v[76:79]\n"
      "ds_read_b128 v[12:15], v174 offset:35840\n"
      "v_mfma_f32_16x16x32_bf16 v[80:83], v[16:19], a[36:39], v[80:83]\n"
      "ds_read_b128 v[16:19], v174 offset:36864\n"
      "s_waitcnt lgkmcnt(7)\n"
      "v_mfma_f32_16x16x32_bf16 v[204:207], v[180:183], a[40:43], v[204:207]\n"
      "ds_read_b128 v[180:183], v174 offset:37888\n"
      "v_mfma_f32_16x16x32_bf16 v[208:211], v[240:243], a[40:43], v[208:211]\n"
      "ds_read_b128 v[240:243], v174 offset:38912\n"
      "v_mfma_f32_16x16x32_bf16 v[44:47], v[252:255], a[40:43], v[44:47]\n"
      "ds_read_b128 v[252:255], v174 offset:39936\n"
      "v_mfma_f32_16x16x32_bf16 v[48:51], v[232:235], a[40:43], v[48:51]\n"
      "ds_read_b128 v[232:235], v174 offset:40960\n"
      "s_add_u32 s96, s83, 56320\n"
      "buffer_load_dwordx4 a[36:39], v239, s[36:39], s96 offen offset:0 sc1\n"
      "v_mfma_f32_16x16x32_bf16 v[52:55], v[156:159], a[40:43], v[52:55]\n"
      "ds_read_b128 v[156:159], v174 offset:41984\n"
      "v_mfma_f32_16x16x32_bf16 v[76:79], v[160:163], a[40:43], v[76:79]\n"
      "ds_read_b128 v[160:163], v174 offset:43008\n"
      "v_mfma_f32_16x16x32_bf16 v[80:83], v[212:215], a[40:43], v[80:83]\n"
      "ds_read_b128 v[212:215], v174 offset:44032\n"
      "s_waitcnt vmcnt(6)\n"
      "buffer_store_dwordx2 v[166:167], v229, s[36:39], 0 offen sc1\n"
      "s_waitcnt lgkmcnt(7)\n"
      "v_mfma_f32_16x16x32_bf16 v[204:207], v[216:219], a[44:47], v[204:207]\n"
      "ds_read_b128 v[216:219], v174 offset:45056\n"
      "v_mfma_f32_16x16x32_bf16 v[208:211], v[220:223], a[44:47], v[208:211]\n"
      "ds_read_b128 v[220:223], v174 offset:46080\n"
      "v_mfma_f32_16x16x32_bf16 v[44:47], v[224:227], a[44:47], v[44:47]\n"
      "ds_read_b128 v[224:227], v174 offset:47104\n"
      "v_mfma_f32_16x16x32_bf16 v[48:51], v[4:7], a[44:47], v[48:51]\n"
      "ds_read_b128 v[4:7], v174 offset:48128\n"
      "buffer_load_dwordx4 a[40:43], v239, s[36:39], s96 offen offset:1024 sc1\n"
      "v_mfma_f32_16x16x32_bf16 v[52:55], v[8:11], a[44:47], v[52:55]\n"
      "ds_read_b128 v[8:11], v174 offset:49152\n"
      "v_mfma_f32_16x16x32_bf16 v[76:79], v[12:15], a[44:47], v[76:79]\n"
      "ds_read_b128 v[12:15], v174 offset:50176\n"
      "v_mfma_f32_16x16x32_bf16 v[80:83], v[16:19], a[44:47], v[80:83]\n"
      "ds_read_b128 v[16:19], v174 offset:51200\n"
      "s_waitcnt lgkmcnt(7)\n"
      "v_mfma_f32_16x16x32_bf16 v[204:207], v[180:183], a[48:51], v[204:207]\n"
      "ds_read_b128 v[180:183], v174 offset:52224\n"
      "v_mfma_f32_16x16x32_bf16 v[208:211], v[240:243], a[48:51], v[208:211]\n"
      "ds_read_b128 v[240:243], v174 offset:53248\n"
      "v_mfma_f32_16x16x32_bf16 v[44:47], v[252:255], a[48:51], v[44:47]\n"
      "ds_read_b128 v[252:255], v174 offset:54272\n"
      "v_mfma_f32_16x16x32_bf16 v[48:51], v[232:235], a[48:51], v[48:51]\n"
      "ds_read_b128 v[232:235], v174 offset:55296\n"
      "s_add_u32 s96, s73, 61440\n"
      "buffer_load_dwordx4 a[44:47], v239, s[36:39], s96 offen offset:0 sc1\n"
      "v_mfma_f32_16x16x32_bf16 v[52:55], v[156:159], a[48:51], v[52:55]\n"
      "ds_read_b128 v[156:159], v174 offset:56320\n"
      "v_mfma_f32_16x16x32_bf16 v[76:79], v[160:163], a[48:51], v[76:79]\n"
      "ds_read_b128 v[160:163], v174 offset:57344\n"
      "v_mfma_f32_16x16x32_bf16 v[80:83], v[212:215], a[48:51], v[80:83]\n"
      "ds_read_b128 v[212:215], v174 offset:58368\n"
      "s_waitcnt lgkmcnt(7)\n"
      "v_mfma_f32_16x16x32_bf16 v[204:207], v[216:219], a[52:55], v[204:207]\n"
      "ds_read_b128 v[216:219], v174 offset:59392\n"
      "v_mfma_f32_16x16x32_bf16 v[208:211], v[220:223], a[52:55], v[208:211]\n"
      "ds_read_b128 v[220:223], v174 offset:60416\n"
      "v_mfma_f32_16x16x32_bf16 v[44:47], v[224:227], a[52:55], v[44:47]\n"
      "ds_read_b128 v[224:227], v178 offset:0\n"
      "v_mfma_f32_16x16x32_bf16 v[48:51], v[4:7], a[52:55], v[48:51]\n"
      "ds_read_b128 v[4:7], v178 offset:1024\n"
      "buffer_load_dwordx4 a[48:51], v239, s[36:39], s96 offen offset:1024 sc1\n"
      "v_mfma_f32_16x16x32_bf16 v[52:55], v[8:11], a[52:55], v[52:55]\n"
      "ds_read_b128 v[8:11], v178 offset:2048\n"
      "v_mfma_f32_16x16x32_bf16 v[76:79], v[12:15], a[52:55], v[76:79]\n"
      "ds_read_b128 v[12:15], v178 offset:3072\n"
      "v_mfma_f32_16x16x32_bf16 v[80:83], v[16:19], a[52:55], v[80:83]\n"
      "ds_read_b128 v[16:19], v178 offset:4096\n"
      "s_waitcnt lgkmcnt(7)\n"
      "v_mfma_f32_16x16x32_bf16 v[204:207], v[180:183], a[56:59], v[204:207]\n"
      "ds_read_b128 v[180:183], v178 offset:5120\n"
      "v_mfma_f32_16x16x32_bf16 v[208:211], v[240:243], a[56:59], v[208:211]\n"
      "ds_read_b128 v[240:243], v178 offset:6144\n"
      "v_mfma_f32_16x16x32_bf16 v[44:47], v[252:255], a[56:59], v[44:47]\n"
      "ds_read_b128 v[252:255], v178 offset:7168\n"
      "v_mfma_f32_16x16x32_bf16 v[48:51], v[232:235], a[56:59], v[48:51]\n"
      "ds_read_b128 v[232:235], v178 offset:8192\n"
      "s_add_u32 s96, s67, 65536\n"
      "buffer_load_dwordx4 a[52:55], v239, s[36:39], s96 offen offset:0 sc1\n"
      "v_mfma_f32_16x16x32_bf16 v[52:55], v[156:159], a[56:59], v[52:55]\n"
      "ds_read_b128 v[156:159], v178 offset:9216\n"
      "v_mfma_f32_16x16x32_bf16 v[76:79], v[160:163], a[56:59], v[76:79]\n"
      "ds_read_b128 v[160:163], v178 offset:10240\n"
      "v_mfma_f32_16x16x32_bf16 v[80:83], v[212:215], a[56:59], v[80:83]\n"
      "ds_read_b128 v[212:215], v178 offset:11264\n"
      "s_waitcnt lgkmcnt(7)\n"
      "v_mfma_f32_16x16x32_bf16 v[204:207], v[216:219], a[60:63], v[204:207]\n"
      "ds_read_b128 v[216:219], v178 offset:12288\n"
      "v_mfma_f32_16x16x32_bf16 v[208:211], v[220:223], a[60:63], v[208:211]\n"
      "ds_read_b128 v[220:223], v178 offset:13312\n"
      "v_mfma_f32_16x16x32_bf16 v[44:47], v[224:227], a[60:63], v[44:47]\n"
      "ds_read_b128 v[224:227], v178 offset:14336\n"
      "v_mfma_f32_16x16x32_bf16 v[48:51], v[4:7], a[60:63], v[48:51]\n"
      "ds_read_b128 v[4:7], v178 offset:15360\n"
      "s_add_u32 s96, s72, 61440\n"
      "buffer_load_dwordx4 a[56:59], v239, s[36:39], s96 offen offset:0 sc1\n"
      "v_mfma_f32_16x16x32_bf16 v[52:55], v[8:11], a[60:63], v[52:55]\n"
      "ds_read_b128 v[8:11], v178 offset:16384\n"
      "v_mfma_f32_16x16x32_bf16 v[76:79], v[12:15], a[60:63], v[76:79]\n"
      "ds_read_b128 v[12:15], v178 offset:17408\n"
      "v_mfma_f32_16x16x32_bf16 v[80:83], v[16:19], a[60:63], v[80:83]\n"
      "ds_read_b128 v[16:19], v178 offset:18432\n"
      "s_waitcnt lgkmcnt(7)\n"
      "v_mfma_f32_16x16x32_bf16 v[204:207], v[180:183], a[64:67], v[204:207]\n"
      "ds_read_b128 v[180:183], v178 offset:19456\n"
      "v_mfma_f32_16x16x32_bf16 v[208:211], v[240:243], a[64:67], v[208:211]\n"
      "ds_read_b128 v[240:243], v178 offset:20480\n"
      "v_mfma_f32_16x16x32_bf16 v[44:47], v[252:255], a[64:67], v[44:47]\n"
      "ds_read_b128 v[252:255], v178 offset:21504\n"
      "v_mfma_f32_16x16x32_bf16 v[48:51], v[232:235], a[64:67], v[48:51]\n"
      "ds_read_b128 v[232:235], v178 offset:22528\n"
      "buffer_load_dwordx4 a[60:63], v239, s[36:39], s96 offen offset:1024 sc1\n"
      "v_mfma_f32_16x16x32_bf16 v[52:55], v[156:159], a[64:67], v[52:55]\n"
      "ds_read_b128 v[156:159], v178 offset:23552\n"
      "v_mfma_f32_16x16x32_bf16 v[76:79], v[160:163], a[64:67], v[76:79]\n"
      "ds_read_b128 v[160:163], v178 offset:24576\n"
      "v_mfma_f32_16x16x32_bf16 v[80:83], v[212:215], a[64:67], v[80:83]\n"
      "ds_read_b128 v[212:215], v178 offset:25600\n"
      "s_waitcnt lgkmcnt(7)\n"
      "v_mfma_f32_16x16x32_bf16 v[204:207], v[216:219], a[68:71], v[204:207]\n"
      "ds_read_b128 v[216:219], v178 offset:26624\n"
      "v_mfma_f32_16x16x32_bf16 v[208:211], v[220:223], a[68:71], v[208:211]\n"
      "ds_read_b128 v[220:223], v178 offset:27648\n"
      "v_mfma_f32_16x16x32_bf16 v[44:47], v[224:227], a[68:71], v[44:47]\n"
      "ds_read_b128 v[224:227], v178 offset:28672\n"
      "v_mfma_f32_16x16x32_bf16 v[48:51], v[4:7], a[68:71], v[48:51]\n"
      "ds_read_b128 v[4:7], v178 offset:29696\n"
      "s_add_u32 s96, s73, 66560\n"
      "buffer_load_dwordx4 a[64:67], v239, s[36:39], s96 offen offset:0 sc1\n"
      "v_mfma_f32_16x16x32_bf16 v[52:55], v[8:11], a[68:71], v[52:55]\n"
      "ds_read_b128 v[8:11], v178 offset:30720\n"
      "v_mfma_f32_16x16x32_bf16 v[76:79], v[12:15], a[68:71], v[76:79]\n"
      "ds_read_b128 v[12:15], v178 offset:31744\n"
      "v_mfma_f32_16x16x32_bf16 v[80:83], v[16:19], a[68:71], v[80:83]\n"
      "ds_read_b128 v[16:19], v178 offset:32768\n"
      "s_waitcnt lgkmcnt(7)\n"
      "v_mfma_f32_16x16x32_bf16 v[204:207], v[180:183], a[72:75], v[204:207]\n"
      "ds_read_b128 v[180:183], v178 offset:33792\n"
      "v_mfma_f32_16x16x32_bf16 v[208:211], v[240:243], a[72:75], v[208:211]\n"
      "ds_read_b128 v[240:243], v178 offset:34816\n"
      "v_mfma_f32_16x16x32_bf16 v[44:47], v[252:255], a[72:75], v[44:47]\n"
      "ds_read_b128 v[252:255], v178 offset:35840\n"
      "v_mfma_f32_16x16x32_bf16 v[48:51], v[232:235], a[72:75], v[48:51]\n"
      "ds_read_b128 v[232:235], v178 offset:36864\n"
      "buffer_load_dwordx4 a[68:71], v239, s[36:39], s96 offen offset:1024 sc1\n"
      "v_mfma_f32_16x16x32_bf16 v[52:55], v[156:159], a[72:75], v[52:55]\n"
      "ds_read_b128 v[156:159], v178 offset:37888\n"
      "v_mfma_f32_16x16x32_bf16 v[76:79], v[160:163], a[72:75], v[76:79]\n"
      "ds_read_b128 v[160:163], v178 offset:38912\n"
      "v_mfma_f32_16x16x32_bf16 v[80:83], v[212:215], a[72:75], v[80:83]\n"
      "ds_read_b128 v[212:215], v178 offset:39936\n"
      "s_waitcnt lgkmcnt(7)\n"
      "v_mfma_f32_16x16x32_bf16 v[204:207], v[216:219], a[76:79], v[204:207]\n"
      "ds_read_b128 v[216:219], v187 offset:0\n"
      "v_mfma_f32_16x16x32_bf16 v[208:211], v[220:223], a[76:79], v[208:211]\n"
      "ds_read_b128 v[220:223], v187 offset:256\n"
      "v_mfma_f32_16x16x32_bf16 v[44:47], v[224:227], a[76:79], v[44:47]\n"
      "ds_read_b128 v[224:227], v187 offset:512\n"
      "v_mfma_f32_16x16x32_bf16 v[48:51], v[4:7], a[76:79], v[48:51]\n"
      "ds_read_b128 v[4:7], v187 offset:768\n"
      "s_add_u32 s96, s67, 70656\n"
      "buffer_load_dwordx4 a[72:75], v239, s[36:39], s96 offen offset:0 sc1\n"
      "v_mfma_f32_16x16x32_bf16 v[52:55], v[8:11], a[76:79], v[52:55]\n"
      "ds_read_b128 v[8:11], v187 offset:1024\n"
      "v_mfma_f32_16x16x32_bf16 v[76:79], v[12:15], a[76:79], v[76:79]\n"
      "ds_read_b128 v[12:15], v187 offset:1280\n"
      "v_mfma_f32_16x16x32_bf16 v[80:83], v[16:19], a[76:79], v[80:83]\n"
      "ds_read_b128 v[16:19], v187 offset:1536\n"
      "s_waitcnt lgkmcnt(7)\n"
      "v_mfma_f32_16x16x32_bf16 v[204:207], v[180:183], a[80:83], v[204:207]\n"
      "v_mfma_f32_16x16x32_bf16 v[208:211], v[240:243], a[80:83], v[208:211]\n"
      "v_mfma_f32_16x16x32_bf16 v[44:47], v[252:255], a[80:83], v[44:47]\n"
      "v_mfma_f32_16x16x32_bf16 v[48:51], v[232:235], a[80:83], v[48:51]\n"
      "s_add_u32 s96, s72, 66560\n"
      "buffer_load_dwordx4 a[76:79], v239, s[36:39], s96 offen offset:0 sc1\n"
      "v_mfma_f32_16x16x32_bf16 v[52:55], v[156:159], a[80:83], v[52:55]\n"
      "v_mfma_f32_16x16x32_bf16 v[76:79], v[160:163], a[80:83], v[76:79]\n"
      "v_mfma_f32_16x16x32_bf16 v[80:83], v[212:215], a[80:83], v[80:83]\n"
      "s_mov_b64 exec, 0xff\n"
      "buffer_load_dwordx2 v[230:231], v186, s[36:39], 0 offen sc1\n"
      "s_mov_b64 exec, -1\n"
      "s_waitcnt lgkmcnt(0)\n"
      "v_mfma_f32_16x16x32_bf16 v[204:207], v[216:219], v[144:147], v[204:207]\n"
      "v_mfma_f32_16x16x32_bf16 v[208:211], v[220:223], v[144:147], v[208:211]\n"
      "v_mfma_f32_16x16x32_bf16 v[44:47], v[224:227], v[144:147], v[44:47]\n"
      "v_mfma_f32_16x16x32_bf16 v[48:51], v[4:7], v[144:147], v[48:51]\n"
      "buffer_load_dwordx4 a[80:83], v239, s[36:39], s96 offen offset:1024 sc1\n"
      "v_mfma_f32_16x16x32_bf16 v[52:55], v[8:11], v[144:147], v[52:55]\n"
      "v_mfma_f32_16x16x32_bf16 v[76:79], v[12:15], v[144:147], v[76:79]\n"
      "v_mfma_f32_16x16x32_bf16 v[80:83], v[16:19], v[144:147], v[80:83]\n"
      "s_nop 0\n"
      "v_pk_mul_f32 v[204:205], v[204:205], v[246:247] op_sel_hi:[1,0]\n"
      "v_pk_mul_f32 v[206:207], v[206:207], v[246:247] op_sel_hi:[1,0]\n"
      "v_cvt_pk_bf16_f32 v168, v204, v205\n"
      "v_cvt_pk_bf16_f32 v169, v206, v207\n"
      "v_pk_max_i16 v168, v168, 0\n"
      "v_pk_max_i16 v169, v169, 0\n"
      "v_pk_mul_f32 v[208:209], v[208:209], v[246:247] op_sel_hi:[1,0]\n"
      "v_pk_mul_f32 v[210:211], v[210:211], v[246:247] op_sel_hi:[1,0]\n"
      "v_cvt_pk_bf16_f32 v170, v208, v209\n"
      "v_cvt_pk_bf16_f32 v171, v210, v211\n"
      "v_pk_max_i16 v170, v170, 0\n"
      "v_pk_max_i16 v171, v171, 0\n"
      "buffer_store_dwordx4 v[168:171], v2, s[36:39], 0 offen offset:0 sc1\n"
      "v_pk_mul_f32 v[44:45], v[44:45], v[246:247] op_sel_hi:[1,0]\n"
      "v_pk_mul_f32 v[46:47], v[46:47], v[246:247] op_sel_hi:[1,0]\n"
      "v_cvt_pk_bf16_f32 v188, v44, v45\n"
      "v_cvt_pk_bf16_f32 v189, v46, v47\n"
      "v_pk_max_i16 v188, v188, 0\n"
      "v_pk_max_i16 v189, v189, 0\n"
      "v_pk_mul_f32 v[48:49], v[48:49], v[246:247] op_sel_hi:[1,0]\n"
      "v_pk_mul_f32 v[50:51], v[50:51], v[246:247] op_sel_hi:[1,0]\n"
      "v_cvt_pk_bf16_f32 v190, v48, v49\n"
      "v_cvt_pk_bf16_f32 v191, v50, v51\n"
      "v_pk_max_i16 v190, v190, 0\n"
      "v_pk_max_i16 v191, v191, 0\n"
      "buffer_store_dwordx4 v[188:191], v2, s[36:39], 0 offen offset:1024 sc1\n"
      "v_pk_mul_f32 v[52:53], v[52:53], v[246:247] op_sel_hi:[1,0]\n"
      "v_pk_mul_f32 v[54:55], v[54:55], v[246:247] op_sel_hi:[1,0]\n"
      "v_cvt_pk_bf16_f32 v168, v52, v53\n"
      "v_cvt_pk_bf16_f32 v169, v54, v55\n"
      "v_pk_max_i16 v168, v168, 0\n"
      "v_pk_max_i16 v169, v169, 0\n"
      "v_pk_mul_f32 v[76:77], v[76:77], v[246:247] op_sel_hi:[1,0]\n"
      "v_pk_mul_f32 v[78:79], v[78:79], v[246:247] op_sel_hi:[1,0]\n"
      "v_cvt_pk_bf16_f32 v170, v76, v77\n"
      "v_cvt_pk_bf16_f32 v171, v78, v79\n"
      "v_pk_max_i16 v170, v170, 0\n"
      "v_pk_max_i16 v171, v171, 0\n"
      "buffer_store_dwordx4 v[168:171], v2, s[36:39], 0 offen offset:2048 sc1\n"
      "v_pk_mul_f32 v[80:81], v[80:81], v[246:247] op_sel_hi:[1,0]\n"
      "v_pk_mul_f32 v[82:83], v[82:83], v[246:247] op_sel_hi:[1,0]\n"
      "v_cvt_pk_bf16_f32 v188, v80, v81\n"
      "v_cvt_pk_bf16_f32 v189, v82, v83\n"
      "v_pk_max_i16 v188, v188, 0\n"
      "v_pk_max_i16 v189, v189, 0\n"
      "s_nop 0\n"
      "v_permlane32_swap_b32 v188, v190\n"
      "v_permlane32_swap_b32 v189, v191\n"
      "buffer_store_dwordx4 v[188:191], v236, s[36:39], 0 offen sc1\n"
      "s_lshr_b32 s92, s101, 1\n"
      "s_lshl_b32 s93, s92, 1\n"
      "s_lshl2_add_u32 s96, s91, s93\n"
      "s_mul_i32 s97, s96, 0xd800\n"
      "s_mul_i32 s65, s101, 0x3800\n"
      "s_add_u32 s66, s97, s65\n"
      "s_add_u32 s94, s66, 0x651000\n"
      "v_add_u32 v203, s94, v3\n"
      "s_sub_u32 s82, 2, s93\n"
      "s_lshl2_add_u32 s83, s91, s82\n"
      "s_mul_i32 s34, s83, 0xd800\n"
      "s_add_u32 s35, s34, s65\n"
      "s_add_u32 s64, s35, 0x651000\n"
      "v_add_u32 v187, s64, v3\n"
      "s_add_u32 s78, s96, 1\n"
      "s_mul_i32 s79, s78, 0xd800\n"
      "s_add_u32 s80, s79, s65\n"
      "s_add_u32 s81, s80, 0x651000\n"
      "v_add_u32 v178, s81, v3\n"
      "s_add_u32 s74, s83, 1\n"
      "s_mul_i32 s75, s74, 0xd800\n"
      "s_add_u32 s76, s75, s65\n"
      "s_add_u32 s77, s76, 0x651000\n"
      "v_add_u32 v2, s77, v3\n"
      "s_add_u32 s73, s67, 0x3000\n"
      "s_mov_b64 exec, 0xff\n"
      "s_waitcnt vmcnt(5)\n"
      "v_cmp_ne_u64 vcc, v[230:231], v[166:167]\n"
      "s_nop 0\n"
      "s_cbranch_vccz L15_%=\n"
      "s_mov_b32 s94, 200000\n"
      "L16_%=:\n"
      "s_sleep 1\n"
      "buffer_load_dwordx2 v[228:229], v186, s[36:39], 0 offen sc1\n"
      "s_waitcnt vmcnt(0)\n"
      "v_cmp_ne_u64 vcc, v[228:229], v[166:167]\n"
      "s_sub_u32 s94, s94, 1\n"
      "s_cbranch_scc1 L17_%=\n"
      "s_cbranch_vccnz L16_%=\n"
      "L17_%=:\n"
      "L15_%=:\n"
      "s_mov_b64 exec, -1\n"
      "buffer_load_dwordx4 v[180:183], v203, s[36:39], 0 offen offset:0 sc1\n"
      "buffer_load_dwordx4 v[168:171], v203, s[36:39], 0 offen offset:1024 sc1\n"
      "buffer_load_dwordx4 v[172:175], v203, s[36:39], 0 offen offset:2048 sc1\n"
      "buffer_load_dwordx4 v[44:47], v203, s[36:39], 0 offen offset:3072 sc1\n"
      "buffer_load_dwordx4 v[48:51], v203, s[36:39], s98 offen offset:0 sc1\n"
      "buffer_load_dwordx4 v[52:55], v203, s[36:39], s98 offen offset:1024 sc1\n"
      "buffer_load_dwordx4 v[188:191], v203, s[36:39], s98 offen offset:2048 sc1\n"
      "buffer_load_dwordx4 v[192:195], v203, s[36:39], s98 offen offset:3072 sc1\n"
      "buffer_load_dwordx4 v[196:199], v203, s[36:39], s90 offen offset:0 sc1\n"
      "buffer_load_dwordx4 v[4:7], v203, s[36:39], s90 offen offset:1024 sc1\n"
      "buffer_load_dwordx4 v[8:11], v203, s[36:39], s90 offen offset:2048 sc1\n"
      "buffer_load_dwordx4 v[12:15], v203, s[36:39], s90 offen offset:3072 sc1\n"
      "s_bitcmp1_b32 s101, 0\n"
      "s_cselect_b32 s96, -1, 0\n"
      "s_mov_b32 s97, s96\n"
      "s_mov_b32 s94, 20480\n"
      "v_cndmask_b32 v236, 0, v64, s[96:97]\n"
      "v_cndmask_b32 v237, 0, v65, s[96:97]\n"
      "v_cndmask_b32 v238, 0, v66, s[96:97]\n"
      "v_cndmask_b32 v239, 0, v67, s[96:97]\n"
      "v_cndmask_b32 v240, 0, v68, s[96:97]\n"
      "v_cndmask_b32 v241, 0, v69, s[96:97]\n"
      "v_cndmask_b32 v242, 0, v70, s[96:97]\n"
      "v_cndmask_b32 v243, 0, v71, s[96:97]\n"
      "v_cndmask_b32 v244, 0, v72, s[96:97]\n"
      "v_cndmask_b32 v245, 0, v73, s[96:97]\n"
      "v_cndmask_b32 v246, 0, v74, s[96:97]\n"
      "v_cndmask_b32 v247, 0, v75, s[96:97]\n"
      "v_cndmask_b32 v248, 0, v56, s[96:97]\n"
      "v_cndmask_b32 v249, 0, v57, s[96:97]\n"
      "v_cndmask_b32 v250, 0, v58, s[96:97]\n"
      "v_cndmask_b32 v251, 0, v59, s[96:97]\n"
      "v_cndmask_b32 v252, 0, v60, s[96:97]\n"
      "v_cndmask_b32 v253, 0, v61, s[96:97]\n"
      "v_cndmask_b32 v254, 0, v62, s[96:97]\n"
      "v_cndmask_b32 v255, 0, v63, s[96:97]\n"
      "v_cndmask_b32 v56, v56, 0, s[96:97]\n"
      "v_cndmask_b32 v57, v57, 0, s[96:97]\n"
      "v_cndmask_b32 v58, v58, 0, s[96:97]\n"
      "v_cndmask_b32 v59, v59, 0, s[96:97]\n"
      "v_cndmask_b32 v60, v60, 0, s[96:97]\n"
      "v_cndmask_b32 v61, v61, 0, s[96:97]\n"
      "v_cndmask_b32 v62, v62, 0, s[96:97]\n"
      "v_cndmask_b32 v63, v63, 0, s[96:97]\n"
      "v_cndmask_b32 v64, v64, 0, s[96:97]\n"
      "v_cndmask_b32 v65, v65, 0, s[96:97]\n"
      "v_cndmask_b32 v66, v66, 0, s[96:97]\n"
      "v_cndmask_b32 v67, v67, 0, s[96:97]\n"
      "v_cndmask_b32 v68, v68, 0, s[96:97]\n"
      "v_cndmask_b32 v69, v69, 0, s[96:97]\n"
      "v_cndmask_b32 v70, v70, 0, s[96:97]\n"
      "v_cndmask_b32 v71, v71, 0, s[96:97]\n"
      "v_cndmask_b32 v72, v72, 0, s[96:97]\n"
      "v_cndmask_b32 v73, v73, 0, s[96:97]\n"
      "v_cndmask_b32 v74, v74, 0, s[96:97]\n"
      "v_cndmask_b32 v75, v75, 0, s[96:97]\n"
      "s_mul_i32 s66, s101, 0x1400\n"
      "s_and_b32 s92, s101, 1\n"
      "s_mul_i32 s93, s92, 0xc00\n"
      "s_add_u32 s72, s66, s93\n"
      "v_add_u32 v201, s72, v3\n"
      "s_add_u32 s75, s66, 0x800\n"
      "v_add_u32 v200, s75, v3\n"
      "s_sub_u32 s65, 0xc00, s93\n"
      "s_add_u32 s74, s66, s65\n"
      "v_add_u32 v185, s74, v3\n"
      "s_lshr_b32 s82, s101, 1\n"
      "s_lshl_b32 s83, s82, 1\n"
      "s_sub_u32 s34, 2, s83\n"
      "s_mul_i32 s35, s34, 0x1400\n"
      "s_add_u32 s64, s35, s93\n"
      "v_add_u32 v184, s64, v3\n"
      "s_add_u32 s81, s35, 0x800\n"
      "v_add_u32 v177, s81, v3\n"
      "s_xor_b32 s78, s101, 1\n"
      "s_mul_i32 s79, s78, 0x1400\n"
      "s_add_u32 s80, s79, s93\n"
      "v_add_u32 v176, s80, v3\n"
      "s_add_u32 s77, s79, 0x800\n"
      "v_add_u32 v165, s77, v3\n"
      "s_and_b32 s70, s89, 1\n"
      "s_lshl_b32 s71, s70, 5\n"
      "s_lshl_b32 s69, s92, 6\n"
      "s_add_u32 s76, s71, s69\n"
      "v_lshrrev_b32 v161, 4, v1\n"
      "v_lshrrev_b32 v162, 1, v161\n"
      "v_and_b32 v159, 1, v161\n"
      "s_lshl_b32 s60, s91, 6\n"
      "s_add_u32 s61, s60, 32\n"
      "s_lshl_b32 s59, s82, 5\n"
      "s_sub_u32 s62, s61, s59\n"
      "s_mul_i32 s63, s62, 0x500\n"
      "s_mul_i32 s58, s89, 0xa0\n"
      "s_add_u32 s68, s63, s58\n"
      "v_and_b32 v156, 15, v1\n"
      "v_mul_u32_u24 v157, 0x500, v156\n"
      "v_add_u32 v158, s68, v157\n"
      "v_lshl_add_u32 v160, v159, 5, v158\n"
      "v_lshl_add_u32 v163, v162, 4, v160\n"
      "v_add_u32 v164, s76, v163\n"
      "s_lshl_b32 s55, s70, 7\n"
      "s_sub_u32 s56, 0x80, s55\n"
      "s_lshl_b32 s54, s92, 30\n"
      "s_add_u32 s57, s56, s54\n"
      "v_lshl_add_u32 v154, v161, 3, v158\n"
      "v_add_u32 v155, s57, v154\n"
      "s_barrier\n"
      "s_waitcnt vmcnt(12)\n"
      "buffer_store_dwordx2 v[166:167], v202, s[36:39], 0 offen sc1\n"
      "s_waitcnt vmcnt(12)\n"
      "v_mfma_f32_16x16x32_bf16 v[56:59], a[148:151], v[180:183], v[56:59]\n"
      "buffer_load_dwordx4 v[16:19], v203, s[36:39], s73 offen offset:0 sc1\n"
      "v_mfma_f32_16x16x32_bf16 v[60:63], a[152:155], v[180:183], v[60:63]\n"
      "v_mfma_f32_16x16x32_bf16 v[72:75], a[156:159], v[180:183], v[72:75]\n"
      "buffer_load_dwordx4 v[204:207], v203, s[36:39], s73 offen offset:1024 sc1\n"
      "v_mfma_f32_16x16x32_bf16 v[64:67], a[160:163], v[180:183], v[64:67]\n"
      "v_mfma_f32_16x16x32_bf16 v[68:71], a[164:167], v[180:183], v[68:71]\n"
      "buffer_load_dwordx4 v[208:211], v187, s[36:39], 0 offen offset:0 sc1\n"
      "s_waitcnt vmcnt(14)\n"
      "v_mfma_f32_16x16x32_bf16 v[56:59], a[168:171], v[168:171], v[56:59]\n"
      "buffer_load_dwordx4 v[212:215], v187, s[36:39], 0 offen offset:1024 sc1\n"
      "v_mfma_f32_16x16x32_bf16 v[60:63], a[172:175], v[168:171], v[60:63]\n"
      "v_mfma_f32_16x16x32_bf16 v[72:75], a[176:179], v[168:171], v[72:75]\n"
      "buffer_load_dwordx4 v[180:183], v187, s[36:39], 0 offen offset:2048 sc1\n"
      "v_mfma_f32_16x16x32_bf16 v[64:67], a[180:183], v[168:171], v[64:67]\n"
      "v_mfma_f32_16x16x32_bf16 v[68:71], a[184:187], v[168:171], v[68:71]\n"
      "buffer_load_dwordx4 v[168:171], v187, s[36:39], 0 offen offset:3072 sc1\n"
      "s_waitcnt vmcnt(16)\n"
      "v_mfma_f32_16x16x32_bf16 v[56:59], a[188:191], v[172:175], v[56:59]\n"
      "v_mfma_f32_16x16x32_bf16 v[60:63], a[192:195], v[172:175], v[60:63]\n"
      "v_mfma_f32_16x16x32_bf16 v[72:75], a[196:199], v[172:175], v[72:75]\n"
      "v_mfma_f32_16x16x32_bf16 v[64:67], a[200:203], v[172:175], v[64:67]\n"
      "v_mfma_f32_16x16x32_bf16 v[68:71], a[204:207], v[172:175], v[68:71]\n"
      "buffer_load_dwordx4 v[172:175], v187, s[36:39], s98 offen offset:0 sc1\n"
      "s_waitcnt vmcnt(16)\n"
      "v_mfma_f32_16x16x32_bf16 v[56:59], a[208:211], v[44:47], v[56:59]\n"
      "v_mfma_f32_16x16x32_bf16 v[60:63], a[212:215], v[44:47], v[60:63]\n"
      "v_mfma_f32_16x16x32_bf16 v[72:75], a[216:219], v[44:47], v[72:75]\n"
      "v_mfma_f32_16x16x32_bf16 v[64:67], a[220:223], v[44:47], v[64:67]\n"
      "v_mfma_f32_16x16x32_bf16 v[68:71], a[224:227], v[44:47], v[68:71]\n"
      "buffer_load_dwordx4 v[44:47], v187, s[36:39], s98 offen offset:1024 sc1\n"
      "s_waitcnt vmcnt(16)\n"
      "v_mfma_f32_16x16x32_bf16 v[56:59], a[228:231], v[48:51], v[56:59]\n"
      "v_mfma_f32_16x16x32_bf16 v[60:63], a[232:235], v[48:51], v[60:63]\n"
      "v_mfma_f32_16x16x32_bf16 v[72:75], a[236:239], v[48:51], v[72:75]\n"
      "v_mfma_f32_16x16x32_bf16 v[64:67], a[240:243], v[48:51], v[64:67]\n"
      "v_mfma_f32_16x16x32_bf16 v[68:71], a[244:247], v[48:51], v[68:71]\n"
      "buffer_load_dwordx4 v[48:51], v187, s[36:39], s98 offen offset:2048 sc1\n"
      "s_waitcnt vmcnt(16)\n"
      "v_mfma_f32_16x16x32_bf16 v[56:59], a[248:251], v[52:55], v[56:59]\n"
      "v_mfma_f32_16x16x32_bf16 v[60:63], a[252:255], v[52:55], v[60:63]\n"
      "v_mfma_f32_16x16x32_bf16 v[72:75], a[84:87], v[52:55], v[72:75]\n"
      "v_mfma_f32_16x16x32_bf16 v[64:67], a[88:91], v[52:55], v[64:67]\n"
      "v_mfma_f32_16x16x32_bf16 v[68:71], a[92:95], v[52:55], v[68:71]\n"
      "buffer_load_dwordx4 v[52:55], v187, s[36:39], s98 offen offset:3072 sc1\n"
      "s_waitcnt vmcnt(16)\n"
      "v_mfma_f32_16x16x32_bf16 v[56:59], a[96:99], v[188:191], v[56:59]\n"
      "v_mfma_f32_16x16x32_bf16 v[60:63], a[100:103], v[188:191], v[60:63]\n"
      "v_mfma_f32_16x16x32_bf16 v[72:75], a[104:107], v[188:191], v[72:75]\n"
      "v_mfma_f32_16x16x32_bf16 v[64:67], a[108:111], v[188:191], v[64:67]\n"
      "v_mfma_f32_16x16x32_bf16 v[68:71], a[112:115], v[188:191], v[68:71]\n"
      "buffer_load_dwordx4 v[188:191], v187, s[36:39], s90 offen offset:0 sc1\n"
      "s_waitcnt vmcnt(16)\n"
      "v_mfma_f32_16x16x32_bf16 v[56:59], a[116:119], v[192:195], v[56:59]\n"
      "v_mfma_f32_16x16x32_bf16 v[60:63], a[120:123], v[192:195], v[60:63]\n"
      "v_mfma_f32_16x16x32_bf16 v[72:75], a[124:127], v[192:195], v[72:75]\n"
      "v_mfma_f32_16x16x32_bf16 v[64:67], a[128:131], v[192:195], v[64:67]\n"
      "v_mfma_f32_16x16x32_bf16 v[68:71], a[132:135], v[192:195], v[68:71]\n"
      "buffer_load_dwordx4 v[192:195], v187, s[36:39], s90 offen offset:1024 sc1\n"
      "s_waitcnt vmcnt(16)\n"
      "v_mfma_f32_16x16x32_bf16 v[56:59], a[136:139], v[196:199], v[56:59]\n"
      "v_mfma_f32_16x16x32_bf16 v[60:63], a[140:143], v[196:199], v[60:63]\n"
      "v_mfma_f32_16x16x32_bf16 v[72:75], a[144:147], v[196:199], v[72:75]\n"
      "v_mfma_f32_16x16x32_bf16 v[64:67], v[20:23], v[196:199], v[64:67]\n"
      "v_mfma_f32_16x16x32_bf16 v[68:71], v[24:27], v[196:199], v[68:71]\n"
      "buffer_load_dwordx4 v[196:199], v187, s[36:39], s90 offen offset:2048 sc1\n"
      "s_waitcnt vmcnt(16)\n"
      "v_mfma_f32_16x16x32_bf16 v[56:59], v[28:31], v[4:7], v[56:59]\n"
      "v_mfma_f32_16x16x32_bf16 v[60:63], v[32:35], v[4:7], v[60:63]\n"
      "v_mfma_f32_16x16x32_bf16 v[72:75], v[36:39], v[4:7], v[72:75]\n"
      "v_mfma_f32_16x16x32_bf16 v[64:67], v[40:43], v[4:7], v[64:67]\n"
      "v_mfma_f32_16x16x32_bf16 v[68:71], a[0:3], v[4:7], v[68:71]\n"
      "buffer_load_dwordx4 v[4:7], v187, s[36:39], s90 offen offset:3072 sc1\n"
      "s_waitcnt vmcnt(16)\n"
      "v_mfma_f32_16x16x32_bf16 v[56:59], a[4:7], v[8:11], v[56:59]\n"
      "v_mfma_f32_16x16x32_bf16 v[60:63], a[8:11], v[8:11], v[60:63]\n"
      "v_mfma_f32_16x16x32_bf16 v[72:75], a[12:15], v[8:11], v[72:75]\n"
      "v_mfma_f32_16x16x32_bf16 v[64:67], a[16:19], v[8:11], v[64:67]\n"
      "v_mfma_f32_16x16x32_bf16 v[68:71], a[20:23], v[8:11], v[68:71]\n"
      "buffer_load_dwordx4 v[8:11], v187, s[36:39], s73 offen offset:0 sc1\n"
      "s_waitcnt vmcnt(16)\n"
      "v_mfma_f32_16x16x32_bf16 v[56:59], a[24:27], v[12:15], v[56:59]\n"
      "v_mfma_f32_16x16x32_bf16 v[60:63], a[28:31], v[12:15], v[60:63]\n"
      "v_mfma_f32_16x16x32_bf16 v[72:75], a[32:35], v[12:15], v[72:75]\n"
      "v_mfma_f32_16x16x32_bf16 v[64:67], a[36:39], v[12:15], v[64:67]\n"
      "v_mfma_f32_16x16x32_bf16 v[68:71], a[40:43], v[12:15], v[68:71]\n"
      "buffer_load_dwordx4 v[12:15], v187, s[36:39], s73 offen offset:1024 sc1\n"
      "s_mov_b64 exec, 0xff\n"
      "buffer_load_dwordx2 v[152:153], v179, s[36:39], 0 offen sc1\n"
      "s_mov_b64 exec, -1\n"
      "s_waitcnt vmcnt(16)\n"
      "v_mfma_f32_16x16x32_bf16 v[56:59], a[44:47], v[16:19], v[56:59]\n"
      "v_mfma_f32_16x16x32_bf16 v[60:63], a[48:51], v[16:19], v[60:63]\n"
      "v_mfma_f32_16x16x32_bf16 v[72:75], a[52:55], v[16:19], v[72:75]\n"
      "v_mfma_f32_16x16x32_bf16 v[64:67], a[56:59], v[16:19], v[64:67]\n"
      "v_mfma_f32_16x16x32_bf16 v[68:71], a[60:63], v[16:19], v[68:71]\n"
      "s_waitcnt vmcnt(15)\n"
      "v_mfma_f32_16x16x32_bf16 v[56:59], a[64:67], v[204:207], v[56:59]\n"
      "v_mfma_f32_16x16x32_bf16 v[60:63], a[68:71], v[204:207], v[60:63]\n"
      "v_mfma_f32_16x16x32_bf16 v[72:75], a[72:75], v[204:207], v[72:75]\n"
      "v_mfma_f32_16x16x32_bf16 v[64:67], a[76:79], v[204:207], v[64:67]\n"
      "v_mfma_f32_16x16x32_bf16 v[68:71], a[80:83], v[204:207], v[68:71]\n"
      "s_waitcnt vmcnt(14)\n"
      "v_mfma_f32_16x16x32_bf16 v[216:219], a[148:151], v[208:211], 0\n"
      "v_mfma_f32_16x16x32_bf16 v[220:223], a[152:155], v[208:211], 0\n"
      "v_mfma_f32_16x16x32_bf16 v[224:227], a[156:159], v[208:211], 0\n"
      "v_mfma_f32_16x16x32_bf16 v[228:231], a[160:163], v[208:211], 0\n"
      "ds_write_b128 v201, v[56:59] offset:0\n"
      "v_mfma_f32_16x16x32_bf16 v[232:235], a[164:167], v[208:211], 0\n"
      "s_waitcnt vmcnt(13)\n"
      "v_mfma_f32_16x16x32_bf16 v[216:219], a[168:171], v[212:215], v[216:219]\n"
      "v_mfma_f32_16x16x32_bf16 v[220:223], a[172:175], v[212:215], v[220:223]\n"
      "v_mfma_f32_16x16x32_bf16 v[224:227], a[176:179], v[212:215], v[224:227]\n"
      "v_mfma_f32_16x16x32_bf16 v[228:231], a[180:183], v[212:215], v[228:231]\n"
      "ds_write_b128 v201, v[60:63] offset:1024\n"
      "v_mfma_f32_16x16x32_bf16 v[232:235], a[184:187], v[212:215], v[232:235]\n"
      "s_waitcnt vmcnt(12)\n"
      "v_mfma_f32_16x16x32_bf16 v[216:219], a[188:191], v[180:183], v[216:219]\n"
      "v_mfma_f32_16x16x32_bf16 v[220:223], a[192:195], v[180:183], v[220:223]\n"
      "v_mfma_f32_16x16x32_bf16 v[224:227], a[196:199], v[180:183], v[224:227]\n"
      "v_mfma_f32_16x16x32_bf16 v[228:231], a[200:203], v[180:183], v[228:231]\n"
      "ds_write_b128 v200, v[72:75] offset:0\n"
      "v_mfma_f32_16x16x32_bf16 v[232:235], a[204:207], v[180:183], v[232:235]\n"
      "s_waitcnt vmcnt(11)\n"
      "v_mfma_f32_16x16x32_bf16 v[216:219], a[208:211], v[168:171], v[216:219]\n"
      "v_mfma_f32_16x16x32_bf16 v[220:223], a[212:215], v[168:171], v[220:223]\n"
      "v_mfma_f32_16x16x32_bf16 v[224:227], a[216:219], v[168:171], v[224:227]\n"
      "v_mfma_f32_16x16x32_bf16 v[228:231], a[220:223], v[168:171], v[228:231]\n"
      "ds_write_b128 v185, v[64:67] offset:0\n"
      "v_mfma_f32_16x16x32_bf16 v[232:235], a[224:227], v[168:171], v[232:235]\n"
      "s_mov_b64 exec, 0xff\n"
      "s_waitcnt vmcnt(0)\n"
      "v_cmp_ne_u64 vcc, v[152:153], v[166:167]\n"
      "s_nop 0\n"
      "s_cbranch_vccz L19_%=\n"
      "s_mov_b32 s72, 200000\n"
      "L20_%=:\n"
      "s_sleep 1\n"
      "buffer_load_dwordx2 v[162:163], v179, s[36:39], 0 offen sc1\n"
      "s_waitcnt vmcnt(0)\n"
      "v_cmp_ne_u64 vcc, v[162:163], v[166:167]\n"
      "s_sub_u32 s72, s72, 1\n"
      "s_cbranch_scc1 L21_%=\n"
      "s_cbranch_vccnz L20_%=\n"
      "L21_%=:\n"
      "L19_%=:\n"
      "s_mov_b64 exec, -1\n"
      "s_nop 0\n"
      "v_mfma_f32_16x16x32_bf16 v[216:219], a[228:231], v[172:175], v[216:219]\n"
      "buffer_load_dwordx4 v[16:19], v178, s[36:39], 0 offen offset:0 sc1\n"
      "v_mfma_f32_16x16x32_bf16 v[220:223], a[232:235], v[172:175], v[220:223]\n"
      "v_mfma_f32_16x16x32_bf16 v[224:227], a[236:239], v[172:175], v[224:227]\n"
      "buffer_load_dwordx4 v[204:207], v178, s[36:39], 0 offen offset:1024 sc1\n"
      "v_mfma_f32_16x16x32_bf16 v[228:231], a[240:243], v[172:175], v[228:231]\n"
      "ds_write_b128 v185, v[68:71] offset:1024\n"
      "v_mfma_f32_16x16x32_bf16 v[232:235], a[244:247], v[172:175], v[232:235]\n"
      "buffer_load_dwordx4 v[208:211], v178, s[36:39], 0 offen offset:2048 sc1\n"
      "v_mfma_f32_16x16x32_bf16 v[216:219], a[248:251], v[44:47], v[216:219]\n"
      "buffer_load_dwordx4 v[212:215], v178, s[36:39], 0 offen offset:3072 sc1\n"
      "v_mfma_f32_16x16x32_bf16 v[220:223], a[252:255], v[44:47], v[220:223]\n"
      "v_mfma_f32_16x16x32_bf16 v[224:227], a[84:87], v[44:47], v[224:227]\n"
      "buffer_load_dwordx4 v[180:183], v178, s[36:39], s98 offen offset:0 sc1\n"
      "v_mfma_f32_16x16x32_bf16 v[228:231], a[88:91], v[44:47], v[228:231]\n"
      "v_mfma_f32_16x16x32_bf16 v[232:235], a[92:95], v[44:47], v[232:235]\n"
      "buffer_load_dwordx4 v[168:171], v178, s[36:39], s98 offen offset:1024 sc1\n"
      "v_mfma_f32_16x16x32_bf16 v[216:219], a[96:99], v[48:51], v[216:219]\n"
      "buffer_load_dwordx4 v[172:175], v178, s[36:39], s98 offen offset:2048 sc1\n"
      "v_mfma_f32_16x16x32_bf16 v[220:223], a[100:103], v[48:51], v[220:223]\n"
      "v_mfma_f32_16x16x32_bf16 v[224:227], a[104:107], v[48:51], v[224:227]\n"
      "buffer_load_dwordx4 v[44:47], v178, s[36:39], s98 offen offset:3072 sc1\n"
      "v_mfma_f32_16x16x32_bf16 v[228:231], a[108:111], v[48:51], v[228:231]\n"
      "v_mfma_f32_16x16x32_bf16 v[232:235], a[112:115], v[48:51], v[232:235]\n"
      "buffer_load_dwordx4 v[48:51], v178, s[36:39], s90 offen offset:0 sc1\n"
      "v_mfma_f32_16x16x32_bf16 v[216:219], a[116:119], v[52:55], v[216:219]\n"
      "v_mfma_f32_16x16x32_bf16 v[220:223], a[120:123], v[52:55], v[220:223]\n"
      "v_mfma_f32_16x16x32_bf16 v[224:227], a[124:127], v[52:55], v[224:227]\n"
      "v_mfma_f32_16x16x32_bf16 v[228:231], a[128:131], v[52:55], v[228:231]\n"
      "v_mfma_f32_16x16x32_bf16 v[232:235], a[132:135], v[52:55], v[232:235]\n"
      "buffer_load_dwordx4 v[52:55], v178, s[36:39], s90 offen offset:1024 sc1\n"
      "v_mfma_f32_16x16x32_bf16 v[216:219], a[136:139], v[188:191], v[216:219]\n"
      "v_mfma_f32_16x16x32_bf16 v[220:223], a[140:143], v[188:191], v[220:223]\n"
      "v_mfma_f32_16x16x32_bf16 v[224:227], a[144:147], v[188:191], v[224:227]\n"
      "v_mfma_f32_16x16x32_bf16 v[228:231], v[20:23], v[188:191], v[228:231]\n"
      "v_mfma_f32_16x16x32_bf16 v[232:235], v[24:27], v[188:191], v[232:235]\n"
      "buffer_load_dwordx4 v[188:191], v178, s[36:39], s90 offen offset:2048 sc1\n"
      "v_mfma_f32_16x16x32_bf16 v[216:219], v[28:31], v[192:195], v[216:219]\n"
      "v_mfma_f32_16x16x32_bf16 v[220:223], v[32:35], v[192:195], v[220:223]\n"
      "v_mfma_f32_16x16x32_bf16 v[224:227], v[36:39], v[192:195], v[224:227]\n"
      "v_mfma_f32_16x16x32_bf16 v[228:231], v[40:43], v[192:195], v[228:231]\n"
      "v_mfma_f32_16x16x32_bf16 v[232:235], a[0:3], v[192:195], v[232:235]\n"
      "buffer_load_dwordx4 v[192:195], v178, s[36:39], s90 offen offset:3072 sc1\n"
      "v_mfma_f32_16x16x32_bf16 v[216:219], a[4:7], v[196:199], v[216:219]\n"
      "v_mfma_f32_16x16x32_bf16 v[220:223], a[8:11], v[196:199], v[220:223]\n"
      "v_mfma_f32_16x16x32_bf16 v[224:227], a[12:15], v[196:199], v[224:227]\n"
      "v_mfma_f32_16x16x32_bf16 v[228:231], a[16:19], v[196:199], v[228:231]\n"
      "v_mfma_f32_16x16x32_bf16 v[232:235], a[20:23], v[196:199], v[232:235]\n"
      "buffer_load_dwordx4 v[196:199], v178, s[36:39], s73 offen offset:0 sc1\n"
      "v_mfma_f32_16x16x32_bf16 v[216:219], a[24:27], v[4:7], v[216:219]\n"
      "v_mfma_f32_16x16x32_bf16 v[220:223], a[28:31], v[4:7], v[220:223]\n"
      "v_mfma_f32_16x16x32_bf16 v[224:227], a[32:35], v[4:7], v[224:227]\n"
      "v_mfma_f32_16x16x32_bf16 v[228:231], a[36:39], v[4:7], v[228:231]\n"
      "v_mfma_f32_16x16x32_bf16 v[232:235], a[40:43], v[4:7], v[232:235]\n"
      "buffer_load_dwordx4 v[4:7], v178, s[36:39], s73 offen offset:1024 sc1\n"
      "v_mfma_f32_16x16x32_bf16 v[216:219], a[44:47], v[8:11], v[216:219]\n"
      "v_mfma_f32_16x16x32_bf16 v[220:223], a[48:51], v[8:11], v[220:223]\n"
      "v_mfma_f32_16x16x32_bf16 v[224:227], a[52:55], v[8:11], v[224:227]\n"
      "v_mfma_f32_16x16x32_bf16 v[228:231], a[56:59], v[8:11], v[228:231]\n"
      "v_mfma_f32_16x16x32_bf16 v[232:235], a[60:63], v[8:11], v[232:235]\n"
      "buffer_load_dwordx4 v[8:11], v2, s[36:39], 0 offen offset:0 sc1\n"
      "v_mfma_f32_16x16x32_bf16 v[216:219], a[64:67], v[12:15], v[216:219]\n"
      "v_mfma_f32_16x16x32_bf16 v[220:223], a[68:71], v[12:15], v[220:223]\n"
      "v_mfma_f32_16x16x32_bf16 v[224:227], a[72:75], v[12:15], v[224:227]\n"
      "v_mfma_f32_16x16x32_bf16 v[228:231], a[76:79], v[12:15], v[228:231]\n"
      "v_mfma_f32_16x16x32_bf16 v[232:235], a[80:83], v[12:15], v[232:235]\n"
      "buffer_load_dwordx4 v[12:15], v2, s[36:39], 0 offen offset:1024 sc1\n"
      "s_waitcnt vmcnt(15)\n"
      "v_mfma_f32_16x16x32_bf16 v[236:239], a[148:151], v[16:19], v[236:239]\n"
      "v_mfma_f32_16x16x32_bf16 v[240:243], a[152:155], v[16:19], v[240:243]\n"
      "v_mfma_f32_16x16x32_bf16 v[244:247], a[156:159], v[16:19], v[244:247]\n"
      "v_mfma_f32_16x16x32_bf16 v[248:251], a[160:163], v[16:19], v[248:251]\n"
      "ds_write_b128 v200, v[224:227] offset:20480\n"
      "v_mfma_f32_16x16x32_bf16 v[252:255], a[164:167], v[16:19], v[252:255]\n"
      "buffer_load_dwordx4 v[16:19], v2, s[36:39], 0 offen offset:2048 sc1\n"
      "s_waitcnt vmcnt(15)\n"
      "v_mfma_f32_16x16x32_bf16 v[236:239], a[168:171], v[204:207], v[236:239]\n"
      "v_mfma_f32_16x16x32_bf16 v[240:243], a[172:175], v[204:207], v[240:243]\n"
      "v_mfma_f32_16x16x32_bf16 v[244:247], a[176:179], v[204:207], v[244:247]\n"
      "v_mfma_f32_16x16x32_bf16 v[248:251], a[180:183], v[204:207], v[248:251]\n"
      "ds_write_b128 v185, v[228:231] offset:20480\n"
      "v_mfma_f32_16x16x32_bf16 v[252:255], a[184:187], v[204:207], v[252:255]\n"
      "buffer_load_dwordx4 v[204:207], v2, s[36:39], 0 offen offset:3072 sc1\n"
      "s_waitcnt vmcnt(15)\n"
      "v_mfma_f32_16x16x32_bf16 v[236:239], a[188:191], v[208:211], v[236:239]\n"
      "v_mfma_f32_16x16x32_bf16 v[240:243], a[192:195], v[208:211], v[240:243]\n"
      "v_mfma_f32_16x16x32_bf16 v[244:247], a[196:199], v[208:211], v[244:247]\n"
      "v_mfma_f32_16x16x32_bf16 v[248:251], a[200:203], v[208:211], v[248:251]\n"
      "ds_write_b128 v185, v[232:235] offset:21504\n"
      "v_mfma_f32_16x16x32_bf16 v[252:255], a[204:207], v[208:211], v[252:255]\n"
      "buffer_load_dwordx4 v[208:211], v2, s[36:39], s98 offen offset:0 sc1\n"
      "s_waitcnt lgkmcnt(0)\n"
      "s_barrier\n"
      "s_waitcnt vmcnt(15)\n"
      "v_mfma_f32_16x16x32_bf16 v[236:239], a[208:211], v[212:215], v[236:239]\n"
      "ds_read_b128 v[156:159], v184 offset:0\n"
      "v_mfma_f32_16x16x32_bf16 v[240:243], a[212:215], v[212:215], v[240:243]\n"
      "ds_read_b128 v[160:163], v184 offset:1024\n"
      "v_mfma_f32_16x16x32_bf16 v[244:247], a[216:219], v[212:215], v[244:247]\n"
      "ds_read_b128 v[76:79], v177 offset:0\n"
      "v_mfma_f32_16x16x32_bf16 v[248:251], a[220:223], v[212:215], v[248:251]\n"
      "ds_read_b128 v[80:83], v184 offset:5120\n"
      "v_mfma_f32_16x16x32_bf16 v[252:255], a[224:227], v[212:215], v[252:255]\n"
      "buffer_load_dwordx4 v[212:215], v2, s[36:39], s98 offen offset:1024 sc1\n"
      "s_waitcnt vmcnt(15)\n"
      "v_mfma_f32_16x16x32_bf16 v[236:239], a[228:231], v[180:183], v[236:239]\n"
      "ds_read_b128 v[84:87], v184 offset:6144\n"
      "v_mfma_f32_16x16x32_bf16 v[240:243], a[232:235], v[180:183], v[240:243]\n"
      "ds_read_b128 v[88:91], v177 offset:5120\n"
      "v_mfma_f32_16x16x32_bf16 v[244:247], a[236:239], v[180:183], v[244:247]\n"
      "ds_read_b128 v[92:95], v176 offset:20480\n"
      "v_mfma_f32_16x16x32_bf16 v[248:251], a[240:243], v[180:183], v[248:251]\n"
      "ds_read_b128 v[96:99], v176 offset:21504\n"
      "v_mfma_f32_16x16x32_bf16 v[252:255], a[244:247], v[180:183], v[252:255]\n"
      "buffer_load_dwordx4 v[180:183], v2, s[36:39], s98 offen offset:2048 sc1\n"
      "s_waitcnt vmcnt(15)\n"
      "v_mfma_f32_16x16x32_bf16 v[236:239], a[248:251], v[168:171], v[236:239]\n"
      "ds_read_b128 v[100:103], v165 offset:20480\n"
      "v_mfma_f32_16x16x32_bf16 v[240:243], a[252:255], v[168:171], v[240:243]\n"
      "s_waitcnt lgkmcnt(8)\n"
      "v_add_f32 v216, v216, v156\n"
      "v_mfma_f32_16x16x32_bf16 v[244:247], a[84:87], v[168:171], v[244:247]\n"
      "v_add_f32 v217, v217, v157\n"
      "v_mfma_f32_16x16x32_bf16 v[248:251], a[88:91], v[168:171], v[248:251]\n"
      "v_add_f32 v218, v218, v158\n"
      "v_mfma_f32_16x16x32_bf16 v[252:255], a[92:95], v[168:171], v[252:255]\n"
      "buffer_load_dwordx4 v[168:171], v2, s[36:39], s98 offen offset:3072 sc1\n"
      "s_waitcnt vmcnt(15)\n"
      "v_mfma_f32_16x16x32_bf16 v[236:239], a[96:99], v[172:175], v[236:239]\n"
      "v_add_f32 v219, v219, v159\n"
      "v_mfma_f32_16x16x32_bf16 v[240:243], a[100:103], v[172:175], v[240:243]\n"
      "s_waitcnt lgkmcnt(7)\n"
      "v_add_f32 v220, v220, v160\n"
      "v_mfma_f32_16x16x32_bf16 v[244:247], a[104:107], v[172:175], v[244:247]\n"
      "v_add_f32 v221, v221, v161\n"
      "v_mfma_f32_16x16x32_bf16 v[248:251], a[108:111], v[172:175], v[248:251]\n"
      "v_add_f32 v222, v222, v162\n"
      "v_mfma_f32_16x16x32_bf16 v[252:255], a[112:115], v[172:175], v[252:255]\n"
      "buffer_load_dwordx4 v[172:175], v2, s[36:39], s90 offen offset:0 sc1\n"
      "s_waitcnt vmcnt(15)\n"
      "v_mfma_f32_16x16x32_bf16 v[236:239], a[116:119], v[44:47], v[236:239]\n"
      "v_add_f32 v223, v223, v163\n"
      "v_mfma_f32_16x16x32_bf16 v[240:243], a[120:123], v[44:47], v[240:243]\n"
      "s_waitcnt lgkmcnt(6)\n"
      "v_add_f32 v224, v224, v76\n"
      "v_mfma_f32_16x16x32_bf16 v[244:247], a[124:127], v[44:47], v[244:247]\n"
      "v_add_f32 v225, v225, v77\n"
      "v_mfma_f32_16x16x32_bf16 v[248:251], a[128:131], v[44:47], v[248:251]\n"
      "v_add_f32 v226, v226, v78\n"
      "v_mfma_f32_16x16x32_bf16 v[252:255], a[132:135], v[44:47], v[252:255]\n"
      "buffer_load_dwordx4 v[44:47], v2, s[36:39], s90 offen offset:1024 sc1\n"
      "s_waitcnt vmcnt(15)\n"
      "v_mfma_f32_16x16x32_bf16 v[236:239], a[136:139], v[48:51], v[236:239]\n"
      "v_add_f32 v227, v227, v79\n"
      "v_mfma_f32_16x16x32_bf16 v[240:243], a[140:143], v[48:51], v[240:243]\n"
      "s_waitcnt lgkmcnt(2)\n"
      "v_add_f32 v80, v80, v92\n"
      "v_mfma_f32_16x16x32_bf16 v[244:247], a[144:147], v[48:51], v[244:247]\n"
      "v_add_f32 v81, v81, v93\n"
      "v_mfma_f32_16x16x32_bf16 v[248:251], v[20:23], v[48:51], v[248:251]\n"
      "v_add_f32 v82, v82, v94\n"
      "v_mfma_f32_16x16x32_bf16 v[252:255], v[24:27], v[48:51], v[252:255]\n"
      "buffer_load_dwordx4 v[48:51], v2, s[36:39], s90 offen offset:2048 sc1\n"
      "s_waitcnt vmcnt(15)\n"
      "v_mfma_f32_16x16x32_bf16 v[236:239], v[28:31], v[52:55], v[236:239]\n"
      "v_add_f32 v83, v83, v95\n"
      "v_mfma_f32_16x16x32_bf16 v[240:243], v[32:35], v[52:55], v[240:243]\n"
      "s_waitcnt lgkmcnt(1)\n"
      "v_add_f32 v84, v84, v96\n"
      "v_mfma_f32_16x16x32_bf16 v[244:247], v[36:39], v[52:55], v[244:247]\n"
      "v_add_f32 v85, v85, v97\n"
      "v_mfma_f32_16x16x32_bf16 v[248:251], v[40:43], v[52:55], v[248:251]\n"
      "v_add_f32 v86, v86, v98\n"
      "v_mfma_f32_16x16x32_bf16 v[252:255], a[0:3], v[52:55], v[252:255]\n"
      "buffer_load_dwordx4 v[52:55], v2, s[36:39], s90 offen offset:3072 sc1\n"
      "s_waitcnt vmcnt(15)\n"
      "v_mfma_f32_16x16x32_bf16 v[236:239], a[4:7], v[188:191], v[236:239]\n"
      "v_add_f32 v87, v87, v99\n"
      "v_mfma_f32_16x16x32_bf16 v[240:243], a[8:11], v[188:191], v[240:243]\n"
      "s_waitcnt lgkmcnt(0)\n"
      "v_add_f32 v88, v88, v100\n"
      "v_mfma_f32_16x16x32_bf16 v[244:247], a[12:15], v[188:191], v[244:247]\n"
      "v_add_f32 v89, v89, v101\n"
      "v_mfma_f32_16x16x32_bf16 v[248:251], a[16:19], v[188:191], v[248:251]\n"
      "v_add_f32 v90, v90, v102\n"
      "v_mfma_f32_16x16x32_bf16 v[252:255], a[20:23], v[188:191], v[252:255]\n"
      "buffer_load_dwordx4 v[188:191], v2, s[36:39], s73 offen offset:0 sc1\n"
      "s_waitcnt vmcnt(15)\n"
      "v_mfma_f32_16x16x32_bf16 v[236:239], a[24:27], v[192:195], v[236:239]\n"
      "v_add_f32 v91, v91, v103\n"
      "v_mfma_f32_16x16x32_bf16 v[240:243], a[28:31], v[192:195], v[240:243]\n"
      "v_add_f32 v216, v216, v80\n"
      "v_mfma_f32_16x16x32_bf16 v[244:247], a[32:35], v[192:195], v[244:247]\n"
      "v_add_f32 v217, v217, v81\n"
      "v_mfma_f32_16x16x32_bf16 v[248:251], a[36:39], v[192:195], v[248:251]\n"
      "v_add_f32 v218, v218, v82\n"
      "v_mfma_f32_16x16x32_bf16 v[252:255], a[40:43], v[192:195], v[252:255]\n"
      "buffer_load_dwordx4 v[192:195], v2, s[36:39], s73 offen offset:1024 sc1\n"
      "s_waitcnt vmcnt(15)\n"
      "v_mfma_f32_16x16x32_bf16 v[236:239], a[44:47], v[196:199], v[236:239]\n"
      "v_add_f32 v219, v219, v83\n"
      "v_mfma_f32_16x16x32_bf16 v[240:243], a[48:51], v[196:199], v[240:243]\n"
      "v_add_f32 v220, v220, v84\n"
      "v_mfma_f32_16x16x32_bf16 v[244:247], a[52:55], v[196:199], v[244:247]\n"
      "v_add_f32 v221, v221, v85\n"
      "v_mfma_f32_16x16x32_bf16 v[248:251], a[56:59], v[196:199], v[248:251]\n"
      "v_add_f32 v222, v222, v86\n"
      "v_mfma_f32_16x16x32_bf16 v[252:255], a[60:63], v[196:199], v[252:255]\n"
      "v_add_f32 v223, v223, v87\n"
      "s_waitcnt vmcnt(14)\n"
      "v_mfma_f32_16x16x32_bf16 v[236:239], a[64:67], v[4:7], v[236:239]\n"
      "v_add_f32 v224, v224, v88\n"
      "v_mfma_f32_16x16x32_bf16 v[240:243], a[68:71], v[4:7], v[240:243]\n"
      "v_add_f32 v225, v225, v89\n"
      "v_mfma_f32_16x16x32_bf16 v[244:247], a[72:75], v[4:7], v[244:247]\n"
      "v_add_f32 v226, v226, v90\n"
      "v_mfma_f32_16x16x32_bf16 v[248:251], a[76:79], v[4:7], v[248:251]\n"
      "v_add_f32 v227, v227, v91\n"
      "v_mfma_f32_16x16x32_bf16 v[252:255], a[80:83], v[4:7], v[252:255]\n"
      "v_cvt_pk_bf16_f32 v228, v216, v217\n"
      "s_waitcnt vmcnt(13)\n"
      "v_mfma_f32_16x16x32_bf16 v[56:59], a[148:151], v[8:11], 0\n"
      "v_cvt_pk_bf16_f32 v229, v218, v219\n"
      "v_mfma_f32_16x16x32_bf16 v[60:63], a[152:155], v[8:11], 0\n"
      "v_cvt_pk_bf16_f32 v230, v220, v221\n"
      "v_mfma_f32_16x16x32_bf16 v[72:75], a[156:159], v[8:11], 0\n"
      "v_cvt_pk_bf16_f32 v231, v222, v223\n"
      "v_mfma_f32_16x16x32_bf16 v[64:67], a[160:163], v[8:11], 0\n"
      "ds_write_b128 v201, v[236:239] offset:40960\n"
      "v_mfma_f32_16x16x32_bf16 v[68:71], a[164:167], v[8:11], 0\n"
      "v_cvt_pk_bf16_f32 v232, v224, v225\n"
      "s_waitcnt vmcnt(12)\n"
      "v_mfma_f32_16x16x32_bf16 v[56:59], a[168:171], v[12:15], v[56:59]\n"
      "v_cvt_pk_bf16_f32 v233, v226, v227\n"
      "v_mfma_f32_16x16x32_bf16 v[60:63], a[172:175], v[12:15], v[60:63]\n"
      "v_permlane16_swap_b32 v228, v230\n"
      "v_mfma_f32_16x16x32_bf16 v[72:75], a[176:179], v[12:15], v[72:75]\n"
      "v_permlane16_swap_b32 v229, v231\n"
      "v_mfma_f32_16x16x32_bf16 v[64:67], a[180:183], v[12:15], v[64:67]\n"
      "ds_write_b128 v201, v[240:243] offset:41984\n"
      "v_mfma_f32_16x16x32_bf16 v[68:71], a[184:187], v[12:15], v[68:71]\n"
      "buffer_store_dwordx2 v[232:233], v155, s[28:31], 0 offen\n"
      "s_waitcnt vmcnt(12)\n"
      "v_mfma_f32_16x16x32_bf16 v[56:59], a[188:191], v[16:19], v[56:59]\n"
      "buffer_store_dwordx4 v[228:231], v164, s[28:31], 0 offen\n"
      "v_mfma_f32_16x16x32_bf16 v[60:63], a[192:195], v[16:19], v[60:63]\n"
      "v_mfma_f32_16x16x32_bf16 v[72:75], a[196:199], v[16:19], v[72:75]\n"
      "v_mfma_f32_16x16x32_bf16 v[64:67], a[200:203], v[16:19], v[64:67]\n"
      "ds_write_b128 v200, v[244:247] offset:40960\n"
      "v_mfma_f32_16x16x32_bf16 v[68:71], a[204:207], v[16:19], v[68:71]\n"
      "s_waitcnt vmcnt(12)\n"
      "v_mfma_f32_16x16x32_bf16 v[56:59], a[208:211], v[204:207], v[56:59]\n"
      "v_mfma_f32_16x16x32_bf16 v[60:63], a[212:215], v[204:207], v[60:63]\n"
      "v_mfma_f32_16x16x32_bf16 v[72:75], a[216:219], v[204:207], v[72:75]\n"
      "v_mfma_f32_16x16x32_bf16 v[64:67], a[220:223], v[204:207], v[64:67]\n"
      "ds_write_b128 v185, v[248:251] offset:40960\n"
      "v_mfma_f32_16x16x32_bf16 v[68:71], a[224:227], v[204:207], v[68:71]\n"
      "s_waitcnt vmcnt(11)\n"
      "v_mfma_f32_16x16x32_bf16 v[56:59], a[228:231], v[208:211], v[56:59]\n"
      "v_mfma_f32_16x16x32_bf16 v[60:63], a[232:235], v[208:211], v[60:63]\n"
      "v_mfma_f32_16x16x32_bf16 v[72:75], a[236:239], v[208:211], v[72:75]\n"
      "v_mfma_f32_16x16x32_bf16 v[64:67], a[240:243], v[208:211], v[64:67]\n"
      "ds_write_b128 v185, v[252:255] offset:41984\n"
      "v_mfma_f32_16x16x32_bf16 v[68:71], a[244:247], v[208:211], v[68:71]\n"
      "s_waitcnt vmcnt(10)\n"
      "v_mfma_f32_16x16x32_bf16 v[56:59], a[248:251], v[212:215], v[56:59]\n"
      "v_mfma_f32_16x16x32_bf16 v[60:63], a[252:255], v[212:215], v[60:63]\n"
      "v_mfma_f32_16x16x32_bf16 v[72:75], a[84:87], v[212:215], v[72:75]\n"
      "v_mfma_f32_16x16x32_bf16 v[64:67], a[88:91], v[212:215], v[64:67]\n"
      "v_mfma_f32_16x16x32_bf16 v[68:71], a[92:95], v[212:215], v[68:71]\n"
      "s_waitcnt lgkmcnt(0)\n"
      "s_barrier\n"
      "s_waitcnt vmcnt(9)\n"
      "v_mfma_f32_16x16x32_bf16 v[56:59], a[96:99], v[180:183], v[56:59]\n"
      "ds_read_b128 v[80:83], v184 offset:40960\n"
      "v_mfma_f32_16x16x32_bf16 v[60:63], a[100:103], v[180:183], v[60:63]\n"
      "ds_read_b128 v[84:87], v184 offset:41984\n"
      "v_mfma_f32_16x16x32_bf16 v[72:75], a[104:107], v[180:183], v[72:75]\n"
      "ds_read_b128 v[88:91], v177 offset:40960\n"
      "v_mfma_f32_16x16x32_bf16 v[64:67], a[108:111], v[180:183], v[64:67]\n"
      "ds_read_b128 v[92:95], v184 offset:46080\n"
      "v_mfma_f32_16x16x32_bf16 v[68:71], a[112:115], v[180:183], v[68:71]\n"
      "ds_read_b128 v[96:99], v184 offset:47104\n"
      "s_waitcnt vmcnt(8)\n"
      "v_mfma_f32_16x16x32_bf16 v[56:59], a[116:119], v[168:171], v[56:59]\n"
      "ds_read_b128 v[100:103], v177 offset:46080\n"
      "v_mfma_f32_16x16x32_bf16 v[60:63], a[120:123], v[168:171], v[60:63]\n"
      "s_waitcnt lgkmcnt(2)\n"
      "v_add_f32 v80, v80, v92\n"
      "v_mfma_f32_16x16x32_bf16 v[72:75], a[124:127], v[168:171], v[72:75]\n"
      "v_add_f32 v81, v81, v93\n"
      "v_mfma_f32_16x16x32_bf16 v[64:67], a[128:131], v[168:171], v[64:67]\n"
      "v_add_f32 v82, v82, v94\n"
      "v_mfma_f32_16x16x32_bf16 v[68:71], a[132:135], v[168:171], v[68:71]\n"
      "v_add_f32 v83, v83, v95\n"
      "s_waitcnt vmcnt(7)\n"
      "v_mfma_f32_16x16x32_bf16 v[56:59], a[136:139], v[172:175], v[56:59]\n"
      "s_waitcnt lgkmcnt(1)\n"
      "v_add_f32 v84, v84, v96\n"
      "v_mfma_f32_16x16x32_bf16 v[60:63], a[140:143], v[172:175], v[60:63]\n"
      "v_add_f32 v85, v85, v97\n"
      "v_mfma_f32_16x16x32_bf16 v[72:75], a[144:147], v[172:175], v[72:75]\n"
      "v_add_f32 v86, v86, v98\n"
      "v_mfma_f32_16x16x32_bf16 v[64:67], v[20:23], v[172:175], v[64:67]\n"
      "v_add_f32 v87, v87, v99\n"
      "v_mfma_f32_16x16x32_bf16 v[68:71], v[24:27], v[172:175], v[68:71]\n"
      "s_waitcnt lgkmcnt(0)\n"
      "v_add_f32 v88, v88, v100\n"
      "s_waitcnt vmcnt(6)\n"
      "v_mfma_f32_16x16x32_bf16 v[56:59], v[28:31], v[44:47], v[56:59]\n"
      "v_add_f32 v89, v89, v101\n"
      "v_mfma_f32_16x16x32_bf16 v[60:63], v[32:35], v[44:47], v[60:63]\n"
      "v_add_f32 v90, v90, v102\n"
      "v_mfma_f32_16x16x32_bf16 v[72:75], v[36:39], v[44:47], v[72:75]\n"
      "v_add_f32 v91, v91, v103\n"
      "v_mfma_f32_16x16x32_bf16 v[64:67], v[40:43], v[44:47], v[64:67]\n"
      "v_mfma_f32_16x16x32_bf16 v[68:71], a[0:3], v[44:47], v[68:71]\n"
      "s_waitcnt vmcnt(5)\n"
      "v_mfma_f32_16x16x32_bf16 v[56:59], a[4:7], v[48:51], v[56:59]\n"
      "v_mfma_f32_16x16x32_bf16 v[60:63], a[8:11], v[48:51], v[60:63]\n"
      "v_mfma_f32_16x16x32_bf16 v[72:75], a[12:15], v[48:51], v[72:75]\n"
      "v_mfma_f32_16x16x32_bf16 v[64:67], a[16:19], v[48:51], v[64:67]\n"
      "v_mfma_f32_16x16x32_bf16 v[68:71], a[20:23], v[48:51], v[68:71]\n"
      "s_waitcnt vmcnt(4)\n"
      "v_mfma_f32_16x16x32_bf16 v[56:59], a[24:27], v[52:55], v[56:59]\n"
      "v_mfma_f32_16x16x32_bf16 v[60:63], a[28:31], v[52:55], v[60:63]\n"
      "v_mfma_f32_16x16x32_bf16 v[72:75], a[32:35], v[52:55], v[72:75]\n"
      "v_mfma_f32_16x16x32_bf16 v[64:67], a[36:39], v[52:55], v[64:67]\n"
      "v_mfma_f32_16x16x32_bf16 v[68:71], a[40:43], v[52:55], v[68:71]\n"
      "s_waitcnt vmcnt(3)\n"
      "v_mfma_f32_16x16x32_bf16 v[56:59], a[44:47], v[188:191], v[56:59]\n"
      "v_mfma_f32_16x16x32_bf16 v[60:63], a[48:51], v[188:191], v[60:63]\n"
      "v_mfma_f32_16x16x32_bf16 v[72:75], a[52:55], v[188:191], v[72:75]\n"
      "v_mfma_f32_16x16x32_bf16 v[64:67], a[56:59], v[188:191], v[64:67]\n"
      "v_mfma_f32_16x16x32_bf16 v[68:71], a[60:63], v[188:191], v[68:71]\n"
      "s_waitcnt vmcnt(2)\n"
      "v_mfma_f32_16x16x32_bf16 v[64:67], a[76:79], v[192:195], v[64:67]\n"
      "v_mfma_f32_16x16x32_bf16 v[68:71], a[80:83], v[192:195], v[68:71]\n"
      "v_mfma_f32_16x16x32_bf16 v[72:75], a[72:75], v[192:195], v[72:75]\n"
      "v_mfma_f32_16x16x32_bf16 v[56:59], a[64:67], v[192:195], v[56:59]\n"
      "v_mfma_f32_16x16x32_bf16 v[60:63], a[68:71], v[192:195], v[60:63]\n"
      "s_nop 3\n"
      "ds_write_b128 v185, v[64:67] offset:61440\n"
      "ds_write_b128 v185, v[68:71] offset:62464\n"
      "ds_write_b128 v200, v[72:75] offset:61440\n"
      "v_pk_add_f32 v[80:81], v[80:81], v[56:57]\n"
      "v_pk_add_f32 v[82:83], v[82:83], v[58:59]\n"
      "v_pk_add_f32 v[84:85], v[84:85], v[60:61]\n"
      "v_pk_add_f32 v[86:87], v[86:87], v[62:63]\n"
      "v_pk_add_f32 v[88:89], v[88:89], v[72:73]\n"
      "v_pk_add_f32 v[90:91], v[90:91], v[74:75]\n"
      "s_waitcnt lgkmcnt(0)\n"
      "s_barrier\n"
      "ds_read_b128 v[156:159], v176 offset:61440\n"
      "ds_read_b128 v[160:163], v176 offset:62464\n"
      "ds_read_b128 v[76:79], v165 offset:61440\n"
      "s_waitcnt lgkmcnt(2)\n"
      "v_pk_add_f32 v[80:81], v[80:81], v[156:157]\n"
      "v_pk_add_f32 v[82:83], v[82:83], v[158:159]\n"
      "s_waitcnt lgkmcnt(1)\n"
      "v_pk_add_f32 v[84:85], v[84:85], v[160:161]\n"
      "v_pk_add_f32 v[86:87], v[86:87], v[162:163]\n"
      "s_waitcnt lgkmcnt(0)\n"
      "v_pk_add_f32 v[88:89], v[88:89], v[76:77]\n"
      "v_pk_add_f32 v[90:91], v[90:91], v[78:79]\n"
      "v_cvt_pk_bf16_f32 v228, v80, v81\n"
      "v_cvt_pk_bf16_f32 v229, v82, v83\n"
      "v_cvt_pk_bf16_f32 v230, v84, v85\n"
      "v_cvt_pk_bf16_f32 v231, v86, v87\n"
      "v_cvt_pk_bf16_f32 v232, v88, v89\n"
      "v_cvt_pk_bf16_f32 v233, v90, v91\n"
      "v_permlane16_swap_b32 v228, v230\n"
      "v_permlane16_swap_b32 v229, v231\n"
      "buffer_store_dwordx2 v[232:233], v155, s[28:31], s94 offen\n"
      "buffer_store_dwordx4 v[228:231], v164, s[28:31], s94 offen\n"
      "Lend_%=:\n"
      :
      : "s"(__builtin_amdgcn_kernarg_segment_ptr()), "s"(blockIdx.x), "v"(threadIdx.x), "s"((unsigned)(size_t)lds)
      : "v1", "v2", "v3", "v4", "v5", "v6", "v7", "v8", "v9", "v10", "v11", "v12", "v13", "v14", "v15", "v16",
        "v17", "v18", "v19", "v20", "v21", "v22", "v23", "v24", "v25", "v26", "v27", "v28", "v29", "v30", "v31", "v32",
        "v33", "v34", "v35", "v36", "v37", "v38", "v39", "v40", "v41", "v42", "v43", "v44", "v45", "v46", "v47", "v48",
        "v49", "v50", "v51", "v52", "v53", "v54", "v55", "v56", "v57", "v58", "v59", "v60", "v61", "v62", "v63", "v64",
        "v65", "v66", "v67", "v68", "v69", "v70", "v71", "v72", "v73", "v74", "v75", "v76", "v77", "v78", "v79", "v80",
        "v81", "v82", "v83", "v84", "v85", "v86", "v87", "v88", "v89", "v90", "v91", "v92", "v93", "v94", "v95", "v96",
        "v97", "v98", "v99", "v100", "v101", "v102", "v103", "v104", "v105", "v106", "v107", "v108", "v109", "v110", "v111", "v112",
        "v113", "v114", "v115", "v116", "v117", "v118", "v119", "v120", "v121", "v122", "v123", "v124", "v125", "v126", "v127", "v128",
        "v129", "v130", "v131", "v132", "v133", "v134", "v135", "v136", "v137", "v138", "v139", "v140", "v141", "v142", "v143", "v144",
        "v145", "v146", "v147", "v148", "v149", "v150", "v151", "v152", "v153", "v154", "v155", "v156", "v157", "v158", "v159", "v160",
        "v161", "v162", "v163", "v164", "v165", "v166", "v167", "v168", "v169", "v170", "v171", "v172", "v173", "v174", "v175", "v176",
        "v177", "v178", "v179", "v180", "v181", "v182", "v183", "v184", "v185", "v186", "v187", "v188", "v189", "v190", "v191", "v192",
        "v193", "v194", "v195", "v196", "v197", "v198", "v199", "v200", "v201", "v202", "v203", "v204", "v205", "v206", "v207", "v208",
        "v209", "v210", "v211", "v212", "v213", "v214", "v215", "v216", "v217", "v218", "v219", "v220", "v221", "v222", "v223", "v224",
        "v225", "v226", "v227", "v228", "v229", "v230", "v231", "v232", "v233", "v234", "v235", "v236", "v237", "v238", "v239", "v240",
        "v241", "v242", "v243", "v244", "v245", "v246", "v247", "v248", "v249", "v250", "v251", "v252", "v253", "v254", "v255", "a0",
        "a1", "a2", "a3", "a4", "a5", "a6", "a7", "a8", "a9", "a10", "a11", "a12", "a13", "a14", "a15", "a16",
        "a17", "a18", "a19", "a20", "a21", "a22", "a23", "a24", "a25", "a26", "a27", "a28", "a29", "a30", "a31", "a32",
        "a33", "a34", "a35", "a36", "a37", "a38", "a39", "a40", "a41", "a42", "a43", "a44", "a45", "a46", "a47", "a48",
        "a49", "a50", "a51", "a52", "a53", "a54", "a55", "a56", "a57", "a58", "a59", "a60", "a61", "a62", "a63", "a64",
        "a65", "a66", "a67", "a68", "a69", "a70", "a71", "a72", "a73", "a74", "a75", "a76", "a77", "a78", "a79", "a80",
        "a81", "a82", "a83", "a84", "a85", "a86", "a87", "a88", "a89", "a90", "a91", "a92", "a93", "a94", "a95", "a96",
        "a97", "a98", "a99", "a100", "a101", "a102", "a103", "a104", "a105", "a106", "a107", "a108", "a109", "a110", "a111", "a112",
        "a113", "a114", "a115", "a116", "a117", "a118", "a119", "a120", "a121", "a122", "a123", "a124", "a125", "a126", "a127", "a128",
        "a129", "a130", "a131", "a132", "a133", "a134", "a135", "a136", "a137", "a138", "a139", "a140", "a141", "a142", "a143", "a144",
        "a145", "a146", "a147", "a148", "a149", "a150", "a151", "a152", "a153", "a154", "a155", "a156", "a157", "a158", "a159", "a160",
        "a161", "a162", "a163", "a164", "a165", "a166", "a167", "a168", "a169", "a170", "a171", "a172", "a173", "a174", "a175", "a176",
        "a177", "a178", "a179", "a180", "a181", "a182", "a183", "a184", "a185", "a186", "a187", "a188", "a189", "a190", "a191", "a192",
        "a193", "a194", "a195", "a196", "a197", "a198", "a199", "a200", "a201", "a202", "a203", "a204", "a205", "a206", "a207", "a208",
        "a209", "a210", "a211", "a212", "a213", "a214", "a215", "a216", "a217", "a218", "a219", "a220", "a221", "a222", "a223", "a224",
        "a225", "a226", "a227", "a228", "a229", "a230", "a231", "a232", "a233", "a234", "a235", "a236", "a237", "a238", "a239", "a240",
        "a241", "a242", "a243", "a244", "a245", "a246", "a247", "a248", "a249", "a250", "a251", "a252", "a253", "a254", "a255", "s4",
        "s5", "s6", "s7", "s8", "s9", "s10", "s11", "s12", "s13", "s14", "s15", "s16", "s17", "s18", "s19", "s20",
        "s21", "s22", "s23", "s24", "s25", "s26", "s27", "s28", "s29", "s30", "s31", "s34", "s35", "s36", "s37", "s38",
        "s39", "s40", "s41", "s42", "s43", "s44", "s45", "s46", "s47", "s48", "s49", "s50", "s51", "s52", "s53", "s54",
        "s55", "s56", "s57", "s58", "s59", "s60", "s61", "s62", "s63", "s64", "s65", "s66", "s67", "s68", "s69", "s70",
        "s71", "s72", "s73", "s74", "s75", "s76", "s77", "s78", "s79", "s80", "s81", "s82", "s83", "s84", "s85", "s86",
        "s87", "s88", "s89", "s90", "s91", "s92", "s93", "s94", "s95", "s96", "s97", "s98", "s99", "s100", "s101", "vcc",
        "scc", "memory");
#endif
}

extern "C" void kernel_launch(void* const* d_in, const int* in_sizes, int n_in, void* d_out, int out_size, void* d_ws,
                              size_t ws_size, hipStream_t stream) {
  fused_block<<<256, 256, 0, stream>>>(d_in[0], d_in[1], d_in[2], d_in[3], d_in[5], d_in[6], d_out, d_ws);
}
```
